```python
import torch

def _softmax(x, axis=-1):
    return torch.softmax(x, dim=axis)
def _gelu(x):
    return torch.nn.functional.gelu(x, approximate="tanh")
def _silu(x):
    return torch.nn.functional.silu(x)
def _sigmoid(x):
    return torch.sigmoid(x)
def _layernorm(x, axis=-1, eps=1e-5):
    m = x.mean(dim=axis, keepdim=True)
    v = x.var(dim=axis, keepdim=True, unbiased=False)
    return (x - m) / torch.sqrt(v + eps)
def _scatter_add(tgt, idx, src, axis=0):
    return tgt.clone().index_add_(axis, idx, src)
def _segment_sum(src, seg, n):
    out = torch.zeros((n,) + src.shape[1:], dtype=src.dtype)
    return out.index_add_(0, seg, src)
def _pad(x, pw):
    flat = tuple(v for pair in reversed(pw) for v in pair)
    return torch.nn.functional.pad(x, flat)

kernel_name = 'concat_heads__mha_block_cached__mha_block_cached__near_ridge'

def setup_inputs():

    _w = (lambda a: (lambda t: t.to(torch.bfloat16) if t.is_floating_point() else t)(torch.as_tensor(a)))
    def _A_setup():
        B, Sq, D = 8, 144, 512
        H, Sk, Dh = 32, 512, 128
        return {
            "x": _w((lambda *s: torch.randn(*s, dtype=torch.bfloat16) * 0.8)(B, Sq, D)),
            "k_cache": _w((lambda *s: torch.randn(*s, dtype=torch.bfloat16) * 0.8)(B, H, Sk, Dh)),
            "v_cache": _w((lambda *s: torch.randn(*s, dtype=torch.bfloat16) * 0.05)(B, H, Sk, Dh)),
            "wq": _w((lambda *s: torch.randn(*s, dtype=torch.bfloat16) * 0.8)(D, H * Dh) * (D ** -0.5)),
            "wo": _w((lambda *s: torch.randn(*s, dtype=torch.bfloat16) * 0.05)(H * Dh, D) * ((H * Dh) ** -0.5)),
        }
    _A_in = _A_setup()

    def _B_setup():
        B, Sq, D = 8, 144, 512
        H, Sk, Dh = 32, 512, 128
        return {
            "x": _w((lambda *s: torch.randn(*s, dtype=torch.bfloat16) * 0.8)(B, Sq, D)),
            "k_cache": _w((lambda *s: torch.randn(*s, dtype=torch.bfloat16) * 0.8)(B, H, Sk, Dh)),
            "v_cache": _w((lambda *s: torch.randn(*s, dtype=torch.bfloat16) * 0.05)(B, H, Sk, Dh)),
            "wq": _w((lambda *s: torch.randn(*s, dtype=torch.bfloat16) * 0.8)(D, H * Dh) * (D ** -0.5)),
            "wo": _w((lambda *s: torch.randn(*s, dtype=torch.bfloat16) * 0.05)(H * Dh, D) * ((H * Dh) ** -0.5)),
        }
    _B_in = _B_setup()

    return {
        "A_x": _A_in["x"],
        "A_k_cache": _A_in["k_cache"],
        "A_v_cache": _A_in["v_cache"],
        "A_wq": _A_in["wq"],
        "A_wo": _A_in["wo"],
        "B_x": _B_in["x"],
        "B_k_cache": _B_in["k_cache"],
        "B_v_cache": _B_in["v_cache"],
        "B_wq": _B_in["wq"],
        "B_wo": _B_in["wo"],
    }

def reference(A_x, A_k_cache, A_v_cache, A_wq, A_wo, B_x, B_k_cache, B_v_cache, B_wq, B_wo):
    def _A_ref(x, k_cache, v_cache, wq, wo):
        B, Sq, D = x.shape
        H, Dh = 32, 128
        q = (lambda x, *d: x.permute(*d))((x @ wq).reshape(B, Sq, H, Dh), 0, 2, 1, 3)
        scores = torch.einsum("bhqd,bhkd->bhqk", q, k_cache) * (Dh ** -0.5)
        p = _softmax(scores, axis=-1)
        o = torch.einsum("bhqk,bhkd->bhqd", p, v_cache)
        return (lambda x, *d: x.permute(*d))(o, 0, 2, 1, 3).reshape(B, Sq, H * Dh) @ wo

    def _B_ref(x, k_cache, v_cache, wq, wo):
        B, Sq, D = x.shape
        H, Dh = 32, 128
        q = (lambda x, *d: x.permute(*d))((x @ wq).reshape(B, Sq, H, Dh), 0, 2, 1, 3)
        scores = torch.einsum("bhqd,bhkd->bhqk", q, k_cache) * (Dh ** -0.5)
        p = _softmax(scores, axis=-1)
        o = torch.einsum("bhqk,bhkd->bhqd", p, v_cache)
        return (lambda x, *d: x.permute(*d))(o, 0, 2, 1, 3).reshape(B, Sq, H * Dh) @ wo

    a = _A_ref(A_x, A_k_cache, A_v_cache, A_wq, A_wo)
    b = _B_ref(B_x, B_k_cache, B_v_cache, B_wq, B_wo)
    return torch.concatenate([a, b], axis=-1)
```

```cpp
#include <hip/hip_runtime.h>
#include <hip/hip_bf16.h>
#include <stdint.h>
#include <type_traits>

typedef unsigned short u16;
typedef __attribute__((ext_vector_type(8))) short s16x8;
typedef __attribute__((ext_vector_type(4))) short s16x4;
typedef __attribute__((ext_vector_type(4))) float f32x4;
typedef __attribute__((ext_vector_type(2))) float f32x2;
typedef __attribute__((ext_vector_type(4))) unsigned u32x4;
typedef __attribute__((ext_vector_type(2))) unsigned u32x2;
typedef __attribute__((ext_vector_type(2))) __bf16 bf16x2_t;
#define LDS_AS __attribute__((address_space(3)))
#define DI __device__ __forceinline__

constexpr int NB = 8, SQ = 144, DM = 512, NH = 32, SK = 512, DH = 128, HD = NH * DH;
constexpr int SLOT = 16384, NSLOT = 5, LDS_BYTES = SLOT * NSLOT;
constexpr float CEXP = 0.08838834764831845f * 1.4426950408889634f;
constexpr float PSUM_LIMIT = 4096.f;

template <int N, class F> DI void static_for(F&& f) {
  if constexpr (N > 0) { static_for<N - 1>(f); f(std::integral_constant<int, N - 1>{}); }
}

DI f32x4 mfma32(s16x8 a, s16x8 b, f32x4 c) { return __builtin_amdgcn_mfma_f32_16x16x32_bf16(a, b, c, 0, 0, 0); }
DI f32x4 mfma16(s16x4 a, s16x4 b, f32x4 c) { return __builtin_amdgcn_mfma_f32_16x16x16bf16_1k(a, b, c, 0, 0, 0); }
DI s16x8 lds_b128(unsigned a) { return *(const LDS_AS s16x8*)(a); }
DI s16x4 lds_tr(unsigned a) { return __builtin_amdgcn_ds_read_tr16_b64_v4i16((LDS_AS s16x4*)(a)); }
DI s16x8 cat8(s16x4 lo, s16x4 hi) { return __builtin_shufflevector(lo, hi, 0, 1, 2, 3, 4, 5, 6, 7); }
DI unsigned pk2(float a, float b) { f32x2 v = {a, b}; return __builtin_bit_cast(unsigned, __builtin_convertvector(v, bf16x2_t)); }
DI u32x2 pk4(f32x4 v) { u32x2 r = {pk2(v[0], v[1]), pk2(v[2], v[3])}; return r; }

DI u32x4 c2frag(u32x2 t0, u32x2 t1) {
  u32x4 r;
#pragma unroll
  for (int i = 0; i < 2; ++i) {
    auto a = __builtin_amdgcn_permlane32_swap(t0[i], t1[i], false, false);
    auto b = __builtin_amdgcn_permlane16_swap(a[0], a[1], false, false);
    r[i] = b[0]; r[2 + i] = b[1];
  }
  return r;
}
DI float xg_max(float x) {
  auto a = __builtin_amdgcn_permlane16_swap(__float_as_uint(x), __float_as_uint(x), false, false);
  x = fmaxf(__uint_as_float(a[0]), __uint_as_float(a[1]));
  auto b = __builtin_amdgcn_permlane32_swap(__float_as_uint(x), __float_as_uint(x), false, false);
  return fmaxf(__uint_as_float(b[0]), __uint_as_float(b[1]));
}
DI float xg_sum(float x) {
  auto a = __builtin_amdgcn_permlane16_swap(__float_as_uint(x), __float_as_uint(x), false, false);
  x = __uint_as_float(a[0]) + __uint_as_float(a[1]);
  auto b = __builtin_amdgcn_permlane32_swap(__float_as_uint(x), __float_as_uint(x), false, false);
  return __uint_as_float(b[0]) + __uint_as_float(b[1]);
}

DI void dma4(unsigned voff, const char* s0, const char* s1, unsigned l) {
  unsigned keep;
  const unsigned l1 = l + 896, l2 = l + 2048, l3 = l + 2944;
  asm volatile(
      "s_mov_b32 %0, m0\n\t"
      "s_mov_b32 m0, %4\n\t"
      "s_nop 2\n\t"
      "global_load_lds_dwordx4 %1, %2\n\t"
      "s_mov_b32 m0, %5\n\t"
      "s_nop 0\n\t"
      "global_load_lds_dwordx4 %1, %2 offset:128\n\t"
      "s_mov_b32 m0, %6\n\t"
      "s_nop 0\n\t"
      "global_load_lds_dwordx4 %1, %3\n\t"
      "s_mov_b32 m0, %7\n\t"
      "s_nop 0\n\t"
      "global_load_lds_dwordx4 %1, %3 offset:128\n\t"
      "s_mov_b32 m0, %0"
      : "=&s"(keep)
      : "v"(voff), "s"(s0), "s"(s1), "s"(l), "s"(l1), "s"(l2), "s"(l3)
      : "memory");
}
template <int OFF> DI void gld16(s16x8& d, unsigned voff, const char* sb) {
  asm volatile("global_load_dwordx4 %0, %1, %2 offset:%3" : "=v"(d) : "v"(voff), "s"(sb), "i"(OFF));
}
#define WAIT_BAR(N) asm volatile("s_waitcnt vmcnt(" #N ") lgkmcnt(0)\n\ts_barrier" ::: "memory")
template <int N> DI void wait_bar_x(s16x8 (&x)[6]) {
  asm volatile("s_waitcnt vmcnt(%6) lgkmcnt(0)\n\ts_barrier"
               : "+v"(x[0]), "+v"(x[1]), "+v"(x[2]), "+v"(x[3]), "+v"(x[4]), "+v"(x[5]) : "i"(N) : "memory");
}

template <int AST, int WST, class Issue>
DI void gemm144(f32x4 (&acc)[2][8], f32x4 (&accx)[2], const char* a0, const char* a1, const char* a2, const char* wsrc,
                unsigned lds0, int w, int lane, Issue&& issue_next) {
  const int g = lane >> 4, c = lane & 15;
  const unsigned voffA = c * AST + g * 16;
  const int pos = (lane >> 1) & 7;
  const unsigned voffW = ((pos & 3) + 8 * (pos >> 2)) * WST + (lane >> 4) * 32 + (lane & 1) * 16;
  const char* wv = wsrc + (size_t)(16 * w) * WST;
  auto issue_w = [&](int t) {
    const char* s0 = wv + (size_t)(64 * t) * WST;
    dma4(voffW, s0, s0 + 4 * WST, lds0 + (t % NSLOT) * SLOT + w * 4096);
  };
  s16x8 xb[4][6];
  auto issue_x = [&](auto S) {
    constexpr int s = decltype(S)::value;
    gld16<s * 128>(xb[s & 3][0], voffA, a0); gld16<s * 128 + 64>(xb[s & 3][1], voffA, a0);
    gld16<s * 128>(xb[s & 3][2], voffA, a1); gld16<s * 128 + 64>(xb[s & 3][3], voffA, a1);
    gld16<s * 128>(xb[s & 3][4], voffA, a2); gld16<s * 128 + 64>(xb[s & 3][5], voffA, a2);
  };
  static_for<4>([&](auto S) { issue_w(decltype(S)::value); issue_x(S); });
  issue_w(4);
  const unsigned rb = lds0 + (g >> 1) * 4096 + (lane & 31) * 8;
  const unsigned rbx = rb + w * 512;
  static_for<8>([&](auto S) {
    constexpr int s = decltype(S)::value;
    constexpr int N = s == 0 ? 34 : s <= 4 ? 30 : s == 5 ? 24 : s == 6 ? 18 : 12;
    wait_bar_x<N>(xb[s & 3]);
    if constexpr (s >= 1) { if constexpr (s + 4 < 8) issue_w(s + 4); else issue_next(s + 4); }
    constexpr int so = (s % NSLOT) * SLOT;
#pragma unroll
    for (int ks = 0; ks < 2; ++ks) {
#pragma unroll
      for (int dt = 0; dt < 8; ++dt) {
        const s16x8 a = cat8(lds_tr(rb + so + ks * 8192 + dt * 256), lds_tr(rb + so + ks * 8192 + 2048 + dt * 256));
        acc[0][dt] = mfma32(a, xb[s & 3][ks], acc[0][dt]);
        acc[1][dt] = mfma32(a, xb[s & 3][2 + ks], acc[1][dt]);
      }
#pragma unroll
      for (int j = 0; j < 2; ++j) {
        const s16x8 a = cat8(lds_tr(rbx + so + ks * 8192 + j * 256), lds_tr(rbx + so + ks * 8192 + 2048 + j * 256));
        accx[j] = mfma32(a, xb[s & 3][4 + ks], accx[j]);
      }
    }
    asm volatile("" : "+v"(acc[0][0]), "+v"(acc[0][1]), "+v"(acc[0][2]), "+v"(acc[0][3]), "+v"(acc[0][4]), "+v"(acc[0][5]), "+v"(acc[0][6]),
                 "+v"(acc[0][7]), "+v"(acc[1][0]), "+v"(acc[1][1]), "+v"(acc[1][2]), "+v"(acc[1][3]), "+v"(acc[1][4]), "+v"(acc[1][5]),
                 "+v"(acc[1][6]), "+v"(acc[1][7]), "+v"(accx[0]), "+v"(accx[1]));
    if constexpr (s + 4 < 8) issue_x(std::integral_constant<int, s + 4>{});
  });
}

__global__ __launch_bounds__(256, 2) void mha_attn(const u16* __restrict__ xA, const u16* __restrict__ kA, const u16* __restrict__ vA,
                                                   const u16* __restrict__ wqA, const u16* __restrict__ xB, const u16* __restrict__ kB,
                                                   const u16* __restrict__ vB, const u16* __restrict__ wqB, u16* __restrict__ O) {
  extern __shared__ __attribute__((aligned(16))) char smem[];
  const unsigned lds0 = (unsigned)(uintptr_t)(LDS_AS char*)smem;
  const int lane = threadIdx.x & 63;
  const int w = __builtin_amdgcn_readfirstlane(threadIdx.x >> 6);
  const int g = lane >> 4, c = lane & 15;
  const int bid = blockIdx.x, p = bid >> 8, idx = bid & 255;
  const int h = (idx & 7) * 4 + ((idx >> 3) & 3), b = idx >> 5;
  const char* xg = (const char*)(p ? xB : xA) + (size_t)b * SQ * DM * 2;
  const char* wq = (const char*)(p ? wqB : wqA) + h * (DH * 2);
  const char* kg = (const char*)(p ? kB : kA) + (size_t)(b * NH + h) * SK * DH * 2 + w * 4096;
  const char* vg = (const char*)(p ? vB : vA) + (size_t)(b * NH + h) * SK * DH * 2 + w * 4096;
  const unsigned voffK = (lane & 7) * 256 + (lane >> 3) * 16;
  const unsigned voffV = ((lane >> 1) & 7) * 256 + (lane >> 4) * 32 + (lane & 1) * 16;
  auto issue_kv = [&](int i) {
    const int t = (i - 8) >> 1;
    const unsigned l = lds0 + (i % NSLOT) * SLOT + w * 4096;
    if (i & 1) { const char* s = vg + t * SLOT; dma4(voffV, s, s + 2048, l); }
    else { const char* s = kg + t * SLOT; dma4(voffK, s, s + 2048, l); }
  };

  s16x8 q[2][4], qx[4];
  {
    f32x4 acc[2][8], accx[2];
#pragma unroll
    for (int i = 0; i < 8; ++i) { acc[0][i] = f32x4{}; acc[1][i] = f32x4{}; }
    accx[0] = f32x4{}; accx[1] = f32x4{};
    gemm144<DM * 2, HD * 2>(acc, accx, xg + (size_t)(32 * w) * DM * 2, xg + (size_t)(32 * w + 16) * DM * 2, xg + (size_t)128 * DM * 2, wq,
                            lds0, w, lane, issue_kv);
#pragma unroll
    for (int rt = 0; rt < 2; ++rt)
#pragma unroll
      for (int ks = 0; ks < 4; ++ks) q[rt][ks] = __builtin_bit_cast(s16x8, c2frag(pk4(acc[rt][2 * ks]), pk4(acc[rt][2 * ks + 1])));
    const u32x4 fx = c2frag(pk4(accx[0]), pk4(accx[1]));
    WAIT_BAR(63);
    *(LDS_AS u32x4*)(lds0 + 2 * SLOT + w * 1024 + lane * 16) = fx;
  }

  f32x4 o[2][8], ox[8];
#pragma unroll
  for (int i = 0; i < 8; ++i) { o[0][i] = f32x4{}; o[1][i] = f32x4{}; ox[i] = f32x4{}; }
  float m0 = -INFINITY, m1 = -INFINITY, mx = -INFINITY, l0 = 0.f, l1 = 0.f, lx = 0.f;
  const unsigned kb = lds0 + (c >> 3) * 2048 + g * 128 + (c & 7) * 16;
  const unsigned vb = lds0 + (g >> 1) * 2048 + (lane & 31) * 8;
  int sk = 3, sv = 4;
  for (int t = 0; t < 8; ++t) {
    if (t < 7) WAIT_BAR(12); else WAIT_BAR(4);
    if (t == 0) {
#pragma unroll
      for (int ks = 0; ks < 4; ++ks) qx[ks] = lds_b128(lds0 + 2 * SLOT + ks * 1024 + lane * 16);
    } else if (t < 6) issue_kv(12 + 2 * t);
    f32x4 s[2][4], sx = f32x4{};
    {
      const unsigned ka = kb + sk * SLOT, kax = ka + w * 4096;
#pragma unroll
      for (int sb = 0; sb < 4; ++sb) {
        s[0][sb] = f32x4{}; s[1][sb] = f32x4{};
#pragma unroll
        for (int ks = 0; ks < 4; ++ks) {
          const s16x8 kf = lds_b128(ka + sb * 4096 + ks * 512);
          s[0][sb] = mfma32(kf, q[0][ks], s[0][sb]);
          s[1][sb] = mfma32(kf, q[1][ks], s[1][sb]);
        }
      }
#pragma unroll
      for (int ks = 0; ks < 4; ++ks) sx = mfma32(lds_b128(kax + ks * 512), qx[ks], sx);
    }
    u32x2 pp[2][4], ppx;
    float ps0, ps1, psx;
    auto expo = [&]() {
      const float n0 = -m0 * CEXP, n1 = -m1 * CEXP, nx = -mx * CEXP;
      ps0 = 0.f; ps1 = 0.f;
#pragma unroll
      for (int sb = 0; sb < 4; ++sb) {
        f32x4 e0, e1;
#pragma unroll
        for (int r = 0; r < 4; ++r) {
          e0[r] = __builtin_amdgcn_exp2f(fmaf(s[0][sb][r], CEXP, n0));
          e1[r] = __builtin_amdgcn_exp2f(fmaf(s[1][sb][r], CEXP, n1));
        }
        ps0 += (e0[0] + e0[1]) + (e0[2] + e0[3]);
        ps1 += (e1[0] + e1[1]) + (e1[2] + e1[3]);
        pp[0][sb] = pk4(e0); pp[1][sb] = pk4(e1);
      }
      f32x4 ex;
#pragma unroll
      for (int r = 0; r < 4; ++r) ex[r] = __builtin_amdgcn_exp2f(fmaf(sx[r], CEXP, nx));
      psx = (ex[0] + ex[1]) + (ex[2] + ex[3]);
      ppx = pk4(ex);
    };
    bool slow = (t == 0);
    if (!slow) {
      expo();
      slow = __any(!(fmaxf(fmaxf(ps0, ps1), psx) <= PSUM_LIMIT) || ps0 != ps0 || ps1 != ps1 || psx != psx);
    }
    if (slow) {
      float t0 = fmaxf(fmaxf(s[0][0][0], s[0][0][1]), fmaxf(s[0][0][2], s[0][0][3]));
      float t1 = fmaxf(fmaxf(s[1][0][0], s[1][0][1]), fmaxf(s[1][0][2], s[1][0][3]));
#pragma unroll
      for (int sb = 1; sb < 4; ++sb) {
        t0 = fmaxf(t0, fmaxf(fmaxf(s[0][sb][0], s[0][sb][1]), fmaxf(s[0][sb][2], s[0][sb][3])));
        t1 = fmaxf(t1, fmaxf(fmaxf(s[1][sb][0], s[1][sb][1]), fmaxf(s[1][sb][2], s[1][sb][3])));
      }
      float tx = fmaxf(fmaxf(sx[0], sx[1]), fmaxf(sx[2], sx[3]));
      const float n0 = fmaxf(m0, xg_max(t0)), n1 = fmaxf(m1, xg_max(t1)), nx = fmaxf(mx, xg_max(tx));
      const float a0 = __builtin_amdgcn_exp2f((m0 - n0) * CEXP), a1 = __builtin_amdgcn_exp2f((m1 - n1) * CEXP),
                  ax = __builtin_amdgcn_exp2f((mx - nx) * CEXP);
      m0 = n0; m1 = n1; mx = nx;
      l0 *= a0; l1 *= a1; lx *= ax;
#pragma unroll
      for (int i = 0; i < 8; ++i) { o[0][i] *= a0; o[1][i] *= a1; ox[i] *= ax; }
      expo();
    }
    l0 += ps0; l1 += ps1; lx += psx;
    if (t == 0 || t == 6) WAIT_BAR(8); else if (t == 7) WAIT_BAR(0); else WAIT_BAR(12);
    if (t == 0) issue_kv(12);
    if (t < 6) issue_kv(13 + 2 * t);
    {
      const unsigned va = vb + sv * SLOT, vax = va + w * 4096;
      const s16x8 p00 = __builtin_bit_cast(s16x8, (u32x4){pp[0][0][0], pp[0][0][1], pp[0][1][0], pp[0][1][1]});
      const s16x8 p01 = __builtin_bit_cast(s16x8, (u32x4){pp[0][2][0], pp[0][2][1], pp[0][3][0], pp[0][3][1]});
      const s16x8 p10 = __builtin_bit_cast(s16x8, (u32x4){pp[1][0][0], pp[1][0][1], pp[1][1][0], pp[1][1][1]});
      const s16x8 p11 = __builtin_bit_cast(s16x8, (u32x4){pp[1][2][0], pp[1][2][1], pp[1][3][0], pp[1][3][1]});
      const s16x4 px = __builtin_bit_cast(s16x4, ppx);
#pragma unroll
      for (int dt = 0; dt < 8; ++dt) {
        const s16x8 v0 = cat8(lds_tr(va + dt * 256), lds_tr(va + 4096 + dt * 256));
        const s16x8 v1 = cat8(lds_tr(va + 8192 + dt * 256), lds_tr(va + 12288 + dt * 256));
        o[0][dt] = mfma32(v0, p00, o[0][dt]);
        o[1][dt] = mfma32(v0, p10, o[1][dt]);
        o[0][dt] = mfma32(v1, p01, o[0][dt]);
        o[1][dt] = mfma32(v1, p11, o[1][dt]);
        ox[dt] = mfma16(lds_tr(vax + dt * 256), px, ox[dt]);
      }
    }
    sk += 2; if (sk >= NSLOT) sk -= NSLOT;
    sv += 2; if (sv >= NSLOT) sv -= NSLOT;
  }

  char* og = (char*)O + ((size_t)(p * NB + b) * SQ) * (HD * 2) + h * (DH * 2);
  {
    const float r0 = 1.f / xg_sum(l0), r1 = 1.f / xg_sum(l1);
    char* orow = og + (size_t)(32 * w + c) * (HD * 2) + g * 16;
#pragma unroll
    for (int blk = 0; blk < 4; ++blk) {
      *(u32x4*)(orow + blk * 64) = c2frag(pk4(o[0][2 * blk] * r0), pk4(o[0][2 * blk + 1] * r0));
      *(u32x4*)(orow + (size_t)16 * (HD * 2) + blk * 64) = c2frag(pk4(o[1][2 * blk] * r1), pk4(o[1][2 * blk + 1] * r1));
    }
  }
  lx = xg_sum(lx);
  WAIT_BAR(63);
  {
    const unsigned pb = lds0 + w * 8192 + lane * 16;
#pragma unroll
    for (int dt = 0; dt < 8; ++dt) *(LDS_AS f32x4*)(pb + dt * 1024) = ox[dt];
    if (g == 0) { *(LDS_AS f32x2*)(lds0 + 32768 + w * 128 + c * 8) = (f32x2){mx, lx}; }
  }
  WAIT_BAR(63);
  {
    f32x2 ml[4];
#pragma unroll
    for (int i = 0; i < 4; ++i) ml[i] = *(const LDS_AS f32x2*)(lds0 + 32768 + i * 128 + c * 8);
    const float M = fmaxf(fmaxf(ml[0][0], ml[1][0]), fmaxf(ml[2][0], ml[3][0]));
    float sc[4], L = 0.f;
#pragma unroll
    for (int i = 0; i < 4; ++i) { sc[i] = __builtin_amdgcn_exp2f((ml[i][0] - M) * CEXP); L += sc[i] * ml[i][1]; }
    const float rl = 1.f / L;
    f32x4 a0 = f32x4{}, a1 = f32x4{};
#pragma unroll
    for (int i = 0; i < 4; ++i) {
      const unsigned pb = lds0 + i * 8192 + (2 * w) * 1024 + lane * 16;
      a0 += *(const LDS_AS f32x4*)(pb) * sc[i];
      a1 += *(const LDS_AS f32x4*)(pb + 1024) * sc[i];
    }
    *(u32x4*)(og + (size_t)(128 + c) * (HD * 2) + w * 64 + g * 16) = c2frag(pk4(a0 * rl), pk4(a1 * rl));
  }
}

__global__ __launch_bounds__(256, 2) void mha_oproj(const u16* __restrict__ O, const u16* __restrict__ woA, const u16* __restrict__ woB,
                                                    float* __restrict__ slab) {
  extern __shared__ __attribute__((aligned(16))) char smem[];
  const unsigned lds0 = (unsigned)(uintptr_t)(LDS_AS char*)smem;
  const int lane = threadIdx.x & 63;
  const int w = __builtin_amdgcn_readfirstlane(threadIdx.x >> 6);
  const int g = lane >> 4, c = lane & 15;
  const int bid = blockIdx.x, p = bid >> 8, idx = bid & 255;
  const int ksl = idx & 7, nt = (idx >> 3) & 3, b = idx >> 5;
  const char* ag = (const char*)O + ((size_t)(p * NB + b) * SQ) * (HD * 2) + ksl * 1024;
  const char* wg = (const char*)(p ? woB : woA) + (size_t)(ksl * 512) * (DM * 2) + nt * 256;
  f32x4 acc[2][8], accx[2];
#pragma unroll
  for (int i = 0; i < 8; ++i) { acc[0][i] = f32x4{}; acc[1][i] = f32x4{}; }
  accx[0] = f32x4{}; accx[1] = f32x4{};
  const int pos = (lane >> 1) & 7;
  const unsigned voffW = ((pos & 3) + 8 * (pos >> 2)) * (DM * 2) + (lane >> 4) * 32 + (lane & 1) * 16;
  auto issue_dummy = [&](int i) {
    const char* s0 = wg + (size_t)(16 * w) * (DM * 2);
    dma4(voffW, s0, s0 + 4 * (DM * 2), lds0 + (i % NSLOT) * SLOT + w * 4096);
  };
  gemm144<HD * 2, DM * 2>(acc, accx, ag + (size_t)(32 * w) * (HD * 2), ag + (size_t)(32 * w + 16) * (HD * 2), ag + (size_t)128 * (HD * 2), wg,
                          lds0, w, lane, issue_dummy);
  float* sg = slab + ((size_t)(ksl * 2 + p) * (NB * SQ) + b * SQ) * DM + nt * 128 + g * 4;
#pragma unroll
  for (int rt = 0; rt < 2; ++rt)
#pragma unroll
    for (int dt = 0; dt < 8; ++dt) *(f32x4*)(sg + (size_t)(32 * w + 16 * rt + c) * DM + dt * 16) = acc[rt][dt];
#pragma unroll
  for (int j = 0; j < 2; ++j) *(f32x4*)(sg + (size_t)(128 + c) * DM + (2 * w + j) * 16) = accx[j];
}

__global__ __launch_bounds__(256) void mha_reduce(const float* __restrict__ slab, u16* __restrict__ out) {
  const int i = blockIdx.x * 256 + threadIdx.x;
  const int e = i * 8, p = e / (NB * SQ * DM), rem = e % (NB * SQ * DM), row = rem / DM, n = rem % DM;
  f32x4 a = f32x4{}, b2 = f32x4{};
#pragma unroll
  for (int k = 0; k < 8; ++k) {
    const float* s = slab + (size_t)k * (2 * NB * SQ * DM) + e;
    a += *(const f32x4*)s; b2 += *(const f32x4*)(s + 4);
  }
  const u32x2 lo = pk4(a), hi = pk4(b2);
  *(u32x4*)(out + (size_t)row * (2 * DM) + p * DM + n) = (u32x4){lo[0], lo[1], hi[0], hi[1]};
}

extern "C" void kernel_launch(void* const* d_in, const int* in_sizes, int n_in, void* d_out, int out_size, void* d_ws, size_t ws_size,
                              hipStream_t stream) {
  (void)in_sizes; (void)n_in; (void)out_size; (void)ws_size;
  const u16* const* in = (const u16* const*)d_in;
  u16* O = (u16*)d_ws;
  float* slab = (float*)((char*)d_ws + (size_t)2 * NB * SQ * HD * 2);
  (void)hipFuncSetAttribute((const void*)mha_attn, hipFuncAttributeMaxDynamicSharedMemorySize, LDS_BYTES);
  (void)hipFuncSetAttribute((const void*)mha_oproj, hipFuncAttributeMaxDynamicSharedMemorySize, LDS_BYTES);
  mha_attn<<<512, 256, LDS_BYTES, stream>>>(in[0], in[1], in[2], in[3], in[5], in[6], in[7], in[8], O);
  mha_oproj<<<512, 256, LDS_BYTES, stream>>>(O, in[4], in[9], slab);
  mha_reduce<<<(2 * NB * SQ * DM / 8) / 256, 256, 0, stream>>>(slab, (u16*)d_out);
}
```

```cpp
#include <hip/hip_runtime.h>
#include <hip/hip_bf16.h>
#include <stdint.h>
#include <type_traits>

typedef unsigned short u16;
typedef __attribute__((ext_vector_type(8))) short s16x8;
typedef __attribute__((ext_vector_type(4))) short s16x4;
typedef __attribute__((ext_vector_type(16))) float f32x16;
typedef __attribute__((ext_vector_type(4))) float f32x4;
typedef __attribute__((ext_vector_type(2))) float f32x2;
typedef __attribute__((ext_vector_type(4))) unsigned u32x4;
typedef __attribute__((ext_vector_type(2))) unsigned u32x2;
typedef __attribute__((ext_vector_type(2))) __bf16 bf16x2_t;
#define LDS_AS __attribute__((address_space(3)))
#define DI __device__ __forceinline__
#define SB() __builtin_amdgcn_sched_barrier(0)

constexpr int NB = 8, SQ = 144, DM = 512, NH = 32, SK = 512, DH = 128, HD = NH * DH;
constexpr int SLOT = 16384, NSLOT = 5, LDS_BYTES = SLOT * NSLOT;
constexpr float CEXP = 0.08838834764831845f * 1.4426950408889634f;
constexpr float PSUM_LIMIT = 4096.f;

#define STAMP_DECL
#define STAMP_ARG
#define STAMP_PASS
#define STAMP(I) do {} while (0)
#define STAMP_ACC_BEGIN
#define STAMP_ACC_END(I) do {} while (0)
#define STAMP_FLUSH(K) do {} while (0)

#define W_NT false
#define WO_NT false
#define O_NT false
#define ROT1 (b + h)
#define ROT2 (b + 2 * nt)
#define GRID1 512
#define X_NT false
template <int N, class F> DI void static_for(F&& f) {
  if constexpr (N > 0) { static_for<N - 1>(f); f(std::integral_constant<int, N - 1>{}); }
}

DI f32x16 mfmaL(s16x8 a, s16x8 b, f32x16 c) { return __builtin_amdgcn_mfma_f32_32x32x16_bf16(a, b, c, 0, 0, 0); }
DI f32x4 mfma32(s16x8 a, s16x8 b, f32x4 c) { return __builtin_amdgcn_mfma_f32_16x16x32_bf16(a, b, c, 0, 0, 0); }
DI f32x4 mfma16(s16x4 a, s16x4 b, f32x4 c) { return __builtin_amdgcn_mfma_f32_16x16x16bf16_1k(a, b, c, 0, 0, 0); }
DI s16x8 lds_b128(unsigned a) { return *(const LDS_AS s16x8*)(a); }
DI s16x4 lds_tr(unsigned a) { return __builtin_amdgcn_ds_read_tr16_b64_v4i16((LDS_AS s16x4*)(a)); }
DI s16x8 cat8(s16x4 lo, s16x4 hi) { return __builtin_shufflevector(lo, hi, 0, 1, 2, 3, 4, 5, 6, 7); }
DI unsigned pk2(float a, float b) { f32x2 v = {a, b}; return __builtin_bit_cast(unsigned, __builtin_convertvector(v, bf16x2_t)); }
DI u32x2 pk4(f32x4 v) { u32x2 r = {pk2(v[0], v[1]), pk2(v[2], v[3])}; return r; }
DI float ex2(float x) { return __builtin_amdgcn_exp2f(x); }

DI u32x4 c2frag(u32x2 t0, u32x2 t1) {
  u32x4 r;
#pragma unroll
  for (int i = 0; i < 2; ++i) {
    auto a = __builtin_amdgcn_permlane32_swap(t0[i], t1[i], false, false);
    auto b = __builtin_amdgcn_permlane16_swap(a[0], a[1], false, false);
    r[i] = b[0]; r[2 + i] = b[1];
  }
  return r;
}
DI float x32_max(float x) {
  auto b = __builtin_amdgcn_permlane32_swap(__float_as_uint(x), __float_as_uint(x), false, false);
  return fmaxf(__uint_as_float(b[0]), __uint_as_float(b[1]));
}
DI float x32_sum(float x) {
  auto b = __builtin_amdgcn_permlane32_swap(__float_as_uint(x), __float_as_uint(x), false, false);
  return __uint_as_float(b[0]) + __uint_as_float(b[1]);
}
DI float xg_max(float x) {
  auto a = __builtin_amdgcn_permlane16_swap(__float_as_uint(x), __float_as_uint(x), false, false);
  return x32_max(fmaxf(__uint_as_float(a[0]), __uint_as_float(a[1])));
}
DI float xg_sum(float x) {
  auto a = __builtin_amdgcn_permlane16_swap(__float_as_uint(x), __float_as_uint(x), false, false);
  return x32_sum(__uint_as_float(a[0]) + __uint_as_float(a[1]));
}
DI float max16(const f32x16& s) {
  float a = fmaxf(fmaxf(s[0], s[1]), s[2]), b = fmaxf(fmaxf(s[3], s[4]), s[5]);
  a = fmaxf(fmaxf(a, s[6]), s[7]); b = fmaxf(fmaxf(b, s[8]), s[9]);
  a = fmaxf(fmaxf(a, s[10]), s[11]); b = fmaxf(fmaxf(b, s[12]), s[13]);
  return fmaxf(fmaxf(a, s[14]), fmaxf(b, s[15]));
}

template <int GOFF, int LOFF, bool NT>
DI void dma(unsigned voff, const char* sb, unsigned lb) {
  if constexpr (NT)
    asm volatile("s_add_u32 m0, %2, %3\n\ts_nop 0\n\tglobal_load_lds_dwordx4 %0, %1 offset:%4 nt"
                 ::"v"(voff), "s"(sb), "s"(lb), "i"(LOFF - GOFF), "i"(GOFF) : "memory", "scc", "m0");
  else
    asm volatile("s_add_u32 m0, %2, %3\n\ts_nop 0\n\tglobal_load_lds_dwordx4 %0, %1 offset:%4"
                 ::"v"(voff), "s"(sb), "s"(lb), "i"(LOFF - GOFF), "i"(GOFF) : "memory", "scc", "m0");
}
template <int N> DI void wait_bar() { asm volatile("s_waitcnt vmcnt(%0) lgkmcnt(0)\n\ts_barrier" ::"i"(N) : "memory"); }
template <int N> DI void wait_vm() { asm volatile("s_waitcnt vmcnt(%0)" ::"i"(N) : "memory"); }

DI int tsw(int r) { return (r & 2) | ((r >> 2) & 1); }
constexpr int XBUF = 18432, WBUF = 4 * SLOT;

template <int AST, int ABLK, int WST, bool TAIL, bool WNT, bool XNT, class Tail>
DI void gemm144(f32x4 (&acc)[9][2], const char* ag, const char* wsrc, unsigned lds0, int w, int lane, int rot, Tail&& tail STAMP_ARG) {
  const int g = lane >> 4, c = lane & 15;
  const unsigned va0 = ((lane >> 3) * AST + (((lane & 7) ^ (lane >> 4)) << 4)) ^ ((w & 1) << 6);
  const int pos = (lane >> 2) & 7;
  const unsigned vw0 = (4 * (lane >> 5) + (pos & 3) + 8 * (pos >> 2)) * WST + (((lane & 3) ^ ((pos >> 2) << 1)) << 4);
  unsigned va[5], vw[4];
#pragma unroll
  for (int i = 0; i < 4; ++i) { va[i] = va0 + (8 * w + 32 * i) * AST; vw[i] = vw0 + w * 64 + 16 * i * WST; }
  va[4] = va0 + (128 + 8 * (w & 1)) * AST;
  const unsigned lw = lds0 + WBUF + w * 4096, lx = lds0 + w * 1024, lx4 = lds0 + 16384 + (w & 1) * 1024;
  auto wbase = [&](int t) { return wsrc + (size_t)(64 * ((t + rot) & 7)) * WST; };
  auto xbase = [&](int t) { const int tr = (t + rot) & 7; return ag + (tr >> 1) * ABLK + (tr & 1) * 128; };
  auto issue_w = [&](const char* sb, auto I) {
    constexpr int i = decltype(I)::value;
    dma<0, i * 1024, WNT>(vw[i], sb, lw);
  };
  auto issue_x = [&](const char* sb, auto T, auto I) {
    constexpr int t = decltype(T)::value, i = decltype(I)::value;
    if constexpr (i < 4) dma<0, (t % 3) * XBUF + i * 4096, XNT>(va[i], sb, lx);
    else dma<0, (t % 3) * XBUF, XNT>(va[4], sb, lx4);
  };
  { const char* sb = wbase(0); static_for<4>([&](auto I) { issue_w(sb, I); }); }
  { const char* sb = xbase(0); static_for<5>([&](auto I) { issue_x(sb, std::integral_constant<int, 0>{}, I); }); }
  { const char* sb = xbase(1); static_for<5>([&](auto I) { issue_x(sb, std::integral_constant<int, 1>{}, I); }); }
  const unsigned bx0 = lds0 + (c >> 3) * 1024 + (c & 7) * 128 + ((g ^ (c >> 1)) << 4), bx1 = bx0 ^ 64;
  const unsigned bw0 = lw + (g >> 1) * 1024 + pos * 64 + ((pos >> 2) << 5) + (lane & 3) * 8, bw1 = bw0 ^ 32;
  s16x8 wf[2][2][2];
  auto read_w = [&](s16x8 (&f)[2][2]) {
#pragma unroll
    for (int ks = 0; ks < 2; ++ks) {
      f[ks][0] = cat8(lds_tr(bw0 + ks * 2048), lds_tr(bw0 + ks * 2048 + 512));
      f[ks][1] = cat8(lds_tr(bw1 + ks * 2048), lds_tr(bw1 + ks * 2048 + 512));
    }
  };
  wait_vm<10>(); read_w(wf[0]);
  asm volatile("s_waitcnt lgkmcnt(0)" : "+v"(wf[0][0][0]), "+v"(wf[0][0][1]), "+v"(wf[0][1][0]), "+v"(wf[0][1][1])::"memory");
  { const char* sb = wbase(1); static_for<4>([&](auto I) { issue_w(sb, I); }); }
#define XPD 6
  constexpr int D = XPD;
  static_for<8>([&](auto S) {
    constexpr int s = decltype(S)::value;
    { STAMP_ACC_BEGIN;
    wait_bar<(s == 0) ? 9 : (s == 7) ? (TAIL ? 12 : 0) : 13>();
    STAMP_ACC_END(9); }
    if constexpr (s == 0) STAMP(6);
    if constexpr (s == 4) STAMP(7);
    constexpr int xo = (s % 3) * XBUF;
    const char* sxb = xbase(s + 2);
    const char* swb = wbase(s + 2);
    s16x8 xf[18];
    auto ldx = [&](auto I) {
      constexpr int i = decltype(I)::value;
      if constexpr (i < 18) xf[i] = lds_b128((i >= 9 ? bx1 : bx0) + xo + (i % 9) * 2048);
    };
    static_for<D>(ldx);
    SB();
    static_for<18>([&](auto I) {
      constexpr int i = decltype(I)::value;
      if constexpr (i == 9 && s < 7) {
        wait_vm<(s == 6) ? (TAIL ? 4 : 0) : 5>();
        read_w(wf[(s + 1) & 1]);
      }
      ldx(std::integral_constant<int, i + D>{});
      if constexpr (i < 9 && (i & 1) == 0) {
        if constexpr (s + 2 < 8) issue_x(sxb, std::integral_constant<int, s + 2>{}, std::integral_constant<int, (i >> 1)>{});
        else if constexpr (i < 8 && TAIL) tail(s == 6 ? 0 : 2, std::integral_constant<int, (i >> 1)>{});
      }
      if constexpr (i == 14 && s < 7) {
        constexpr int younger = (18 - D - 10 < 0) ? 0 : ((18 - D - 10 > 5) ? 5 : 18 - D - 10);
        asm volatile("s_waitcnt lgkmcnt(%0)" ::"i"(younger) : "memory");
      }
      if constexpr (i >= 14 && s < 7) {
        if constexpr (s + 2 < 8) issue_w(swb, std::integral_constant<int, i - 14>{}); else if constexpr (TAIL) tail(1, std::integral_constant<int, i - 14>{});
      }
      acc[i % 9][0] = mfma32(wf[s & 1][i / 9][0], xf[i], acc[i % 9][0]);
      acc[i % 9][1] = mfma32(wf[s & 1][i / 9][1], xf[i], acc[i % 9][1]);
      asm volatile("" : "+v"(acc[i % 9][0]), "+v"(acc[i % 9][1]));
      SB();
    });
  });
}

__global__ __launch_bounds__(256, 2) void mha_attn(const u16* __restrict__ xA, const u16* __restrict__ kA, const u16* __restrict__ vA,
                                                   const u16* __restrict__ wqA, const u16* __restrict__ xB, const u16* __restrict__ kB,
                                                   const u16* __restrict__ vB, const u16* __restrict__ wqB, u16* __restrict__ O) {
  extern __shared__ __attribute__((aligned(128))) char smem[];
  const unsigned lds0 = (unsigned)(uintptr_t)(LDS_AS char*)smem;
  const int lane = threadIdx.x & 63;
  const int w = __builtin_amdgcn_readfirstlane(threadIdx.x >> 6);
  const int g = lane >> 4, c = lane & 15, r32 = lane & 31, hh = lane >> 5;
  STAMP_DECL; STAMP(0);
  const int bid = blockIdx.x, p = (bid >> 2) & 1, sl = (bid >> 3) & 31;
  const int h = (bid & 3) * 8 + 2 * (sl & 3) + (bid >> 8), b = sl >> 2;
  const char* xg = (const char*)(p ? xB : xA) + (size_t)b * SQ * DM * 2;
  const char* wq = (const char*)(p ? wqB : wqA) + h * (DH * 2);
  const char* kg = (const char*)(p ? kB : kA) + (size_t)(b * NH + h) * SK * DH * 2 + w * 4096;
  const char* vg = (const char*)(p ? vB : vA) + (size_t)(b * NH + h) * SK * DH * 2 + w * 4096;
  const unsigned vkE = (lane >> 3) * 256 + (((lane & 7) ^ (lane >> 4)) << 4), vkO = vkE ^ 64;
  const unsigned vv = (lane >> 3) * 256 + (((lane & 7) ^ (tsw(lane >> 3) << 1)) << 4);
  auto issue_kv = [&](int j, auto I) {
    constexpr int i = decltype(I)::value;
    const unsigned l = lds0 + ((j + 3) % NSLOT) * SLOT + w * 4096;
    const char* s = ((j & 1) ? vg : kg) + (j >> 1) * SLOT;
    dma<(i & 1) * 128 + (i >> 1) * 2048, i * 1024, true>((j & 1) ? vv : ((i >> 1) ? vkO : vkE), s, l);
  };
  const unsigned kb = lds0 + (r32 >> 3) * 2048 + (r32 & 7) * 128 + ((hh ^ ((r32 >> 1) & 7)) << 4);
  const unsigned kbx = lds0 + w * 4096 + (c >> 3) * 2048 + (c & 7) * 128 + ((g ^ (c >> 1)) << 4);

  s16x8 q[8], qx[4];
  {
    f32x4 acc[9][2];
#pragma unroll
    for (int i = 0; i < 9; ++i) { acc[i][0] = f32x4{}; acc[i][1] = f32x4{}; }
    gemm144<DM * 2, 256, HD * 2, true, W_NT, X_NT>(acc, xg, wq, lds0, w, lane, ROT1, issue_kv STAMP_PASS);
    const unsigned qw = lds0 + SLOT + (c >> 3) * 2048 + (w >> 1) * 1024 + (c & 7) * 128 + (((4 * (w & 1) + g) ^ (c >> 1)) << 4);
    wait_bar<63>();
    STAMP(1);
#pragma unroll
    for (int rt = 0; rt < 8; ++rt) *(LDS_AS u32x4*)(qw + rt * 4096) = c2frag(pk4(acc[rt][0]), pk4(acc[rt][1]));
    const u32x4 fx = c2frag(pk4(acc[8][0]), pk4(acc[8][1]));
    wait_bar<63>();
#pragma unroll
    for (int ks = 0; ks < 8; ++ks) q[ks] = lds_b128((kb ^ ((ks & 3) << 5)) + SLOT + w * 8192 + (ks >> 2) * 1024);
    asm volatile("s_waitcnt lgkmcnt(0)" : "+v"(q[0]), "+v"(q[1]), "+v"(q[2]), "+v"(q[3]), "+v"(q[4]), "+v"(q[5]), "+v"(q[6]), "+v"(q[7])::"memory");
    *(LDS_AS u32x4*)(lds0 + SLOT + w * 8192 + lane * 16) = fx;
  }

  f32x16 o[4];
  f32x4 ox[8];
#pragma unroll
  for (int i = 0; i < 4; ++i) o[i] = f32x16{};
#pragma unroll
  for (int i = 0; i < 8; ++i) ox[i] = f32x4{};
  float m, mx, l = 0.f, lx = 0.f;
  f32x16 sA, sB;
  f32x4 sx;
  u32x4 pA[2], pB[2];
  u32x2 px;
  const int vr = 4 * hh + ((lane >> 2) & 3);
  const unsigned vb = lds0 + vr * 128 + (((g & 1) ^ tsw(vr)) << 5) + (lane & 3) * 8;
  const int vrx = (lane >> 2) & 7;
  const unsigned vbx = lds0 + w * 4096 + (g >> 1) * 2048 + vrx * 128 + (tsw(vrx) << 5) + (lane & 3) * 8;

  s16x8 kf[4], vf[4], kx[4];
  s16x4 vx[4];
  auto ld = [&](auto QK_, auto PV_, auto XM_, auto I, unsigned ko, unsigned vo, unsigned xo) {
    constexpr bool QK = decltype(QK_)::value, PV = decltype(PV_)::value;
    constexpr int XM = decltype(XM_)::value, i = decltype(I)::value;
    if constexpr (QK) kf[i & 3] = lds_b128(((kb + ko) ^ ((i & 3) << 5)) + (i >> 2) * 1024);
    if constexpr (PV) {
      const unsigned a = ((vb + vo) ^ (((i >> 1) & 1) << 6)) + (i & 1) * 4096 + (i >> 2) * 1024;
      vf[i & 3] = cat8(lds_tr(a), lds_tr(a + 2048));
    }
    if constexpr (XM == 1 && i < 4) kx[i] = lds_b128(((kbx + xo) ^ ((i & 1) << 6)) + (i >> 1) * 1024);
    if constexpr (XM == 2 || XM == 4) vx[i & 3] = lds_tr(((vbx + xo) ^ ((i & 3) << 5)) + (i >> 2) * 1024);
  };
  auto stage = [&](auto QK_, auto PV_, auto SM_, auto XM_, auto NQK_, auto NPV_, auto NXM_, auto BAR_, f32x16& sn, const f32x16& sc,
                   const u32x4 (&pc)[2], u32x4 (&pn)[2], unsigned ko, unsigned vo, unsigned xo, unsigned nko, unsigned nvo, unsigned nxo,
                   int jd, bool last) {
    constexpr bool QK = decltype(QK_)::value, PV = decltype(PV_)::value, SM = decltype(SM_)::value;
    constexpr int XM = decltype(XM_)::value, BAR = decltype(BAR_)::value;
    constexpr bool XPV = XM == 2 || XM == 4, XSM = XM == 2 || XM == 3;
    const float nm = -m * CEXP, nmx = -mx * CEXP;
    float ps = 0.f, psx = 0.f, e0 = 0.f, e1 = 0.f, x0 = 0.f, x1 = 0.f;
    u32x2 pxn;
    static_for<8>([&](auto I) {
      constexpr int i = decltype(I)::value;
      if constexpr (i == 6 && BAR >= 0) {
        STAMP_ACC_BEGIN;
        if constexpr (BAR == 0) { wait_bar<0>(); static_for<4>([&](auto J) { issue_kv(3, J); }); }
        else { if (last) wait_bar<0>(); else wait_bar<4>(); }
        STAMP_ACC_END(8);
      }
      if constexpr (QK) sn = mfmaL(kf[i & 3], q[i], i == 0 ? f32x16{} : sn);
      float f0, f1;
      if constexpr (SM) {
        f0 = fmaf(sc[2 * i], CEXP, nm); f1 = fmaf(sc[2 * i + 1], CEXP, nm);
        if constexpr (i > 0) { ps += e0 + e1; pn[(i - 1) >> 2][(i - 1) & 3] = pk2(e0, e1); asm volatile("" : "+v"(pn[(i - 1) >> 2][(i - 1) & 3]), "+v"(ps)); }
        asm volatile("" : "+v"(f0), "+v"(f1));
      }
      SB();
      if constexpr (PV) o[i >> 1] = mfmaL(vf[i & 3], __builtin_bit_cast(s16x8, pc[i & 1]), o[i >> 1]);
      if constexpr (SM) { e0 = ex2(f0); e1 = ex2(f1); asm volatile("" : "+v"(e0), "+v"(e1)); }
      SB();
      if constexpr (XM == 1 && i < 4) sx = mfma32(kx[i], qx[i], i == 0 ? f32x4{} : sx);
      if constexpr (XPV) ox[i] = mfma16(vx[i & 3], __builtin_bit_cast(s16x4, px), ox[i]);
      if constexpr (i < 6) ld(QK_, PV_, XM_, std::integral_constant<int, (i + 2) & 7>{}, ko, vo, xo);
      else ld(NQK_, NPV_, NXM_, std::integral_constant<int, (i + 2) & 7>{}, nko, nvo, nxo);
      if constexpr (XSM && (i == 2 || i == 4)) {
        x0 = ex2(fmaf(sx[i - 2], CEXP, nmx)); x1 = ex2(fmaf(sx[i - 1], CEXP, nmx));
        asm volatile("" : "+v"(x0), "+v"(x1));
      }
      if constexpr (XSM && (i == 3 || i == 5)) {
        psx += x0 + x1; pxn[(i - 3) >> 1] = pk2(x0, x1);
        asm volatile("" : "+v"(pxn[(i - 3) >> 1]), "+v"(psx));
      }
      if constexpr (BAR >= 0 && i >= 6) { if (jd >= 0) issue_kv(jd, std::integral_constant<int, i - 6>{}); }
      if constexpr (XM == 1 && PV && i < 6) { if (jd >= 0) issue_kv(jd + ((i + 2) >> 2), std::integral_constant<int, ((i + 2) & 3)>{}); }
      SB();
    });
    if constexpr (SM) {
      ps += e0 + e1; pn[1][3] = pk2(e0, e1);
      if (__any(!(ps <= PSUM_LIMIT))) {
        const float mn = fmaxf(m, x32_max(max16(sc)));
        const float al = ex2((m - mn) * CEXP), nn = -mn * CEXP;
        m = mn; l *= al; ps = 0.f;
#pragma unroll
        for (int i = 0; i < 4; ++i) o[i] *= al;
#pragma unroll
        for (int i = 0; i < 8; ++i) {
          const float a0 = ex2(fmaf(sc[2 * i], CEXP, nn)), a1 = ex2(fmaf(sc[2 * i + 1], CEXP, nn));
          ps += a0 + a1;
          pn[i >> 2][i & 3] = pk2(a0, a1);
        }
      }
      l += ps;
    }
    if constexpr (XSM) {
      if (__any(!(psx <= PSUM_LIMIT))) {
        const float mn = fmaxf(mx, xg_max(fmaxf(fmaxf(sx[0], sx[1]), fmaxf(sx[2], sx[3]))));
        const float al = ex2((mx - mn) * CEXP), nn = -mn * CEXP;
        mx = mn; lx *= al;
#pragma unroll
        for (int i = 0; i < 8; ++i) ox[i] *= al;
        const float a0 = ex2(fmaf(sx[0], CEXP, nn)), a1 = ex2(fmaf(sx[1], CEXP, nn)), a2 = ex2(fmaf(sx[2], CEXP, nn)),
                    a3 = ex2(fmaf(sx[3], CEXP, nn));
        psx = (a0 + a1) + (a2 + a3);
        pxn[0] = pk2(a0, a1); pxn[1] = pk2(a2, a3);
      }
      lx += psx;
      px = pxn;
    }
  };
  using T = std::true_type; using F = std::false_type;
  using X0 = std::integral_constant<int, 0>; using X1 = std::integral_constant<int, 1>; using X2 = std::integral_constant<int, 2>;
  using X3 = std::integral_constant<int, 3>; using X4 = std::integral_constant<int, 4>; using NB_ = std::integral_constant<int, -1>;

  wait_bar<8>();
  STAMP(2);
#pragma unroll
  for (int ks = 0; ks < 4; ++ks) qx[ks] = lds_b128(lds0 + SLOT + ks * 8192 + lane * 16);
  m = 0.f; mx = 0.f;
  ld(T{}, F{}, X1{}, X0{}, 3 * SLOT, 0, 3 * SLOT); ld(T{}, F{}, X1{}, X1{}, 3 * SLOT, 0, 3 * SLOT);
  SB();
  stage(T{}, F{}, F{}, X1{}, T{}, F{}, X3{}, NB_{}, sA, sB, pB, pA, 3 * SLOT, 0, 3 * SLOT, 3 * SLOT + 8192, 0, 0, -1, false);
  m = x32_max(max16(sA));
  mx = xg_max(fmaxf(fmaxf(sx[0], sx[1]), fmaxf(sx[2], sx[3])));
  stage(T{}, F{}, T{}, X3{}, T{}, T{}, X1{}, X0{}, sB, sA, pB, pA, 3 * SLOT + 8192, 0, 0, 0, 4 * SLOT, 0, 4, false);
  int sv = 4, sk = 0;
  for (int t = 0; t < 6; ++t) {
    if (t == 3) STAMP(3);
    int nk = sk + 2, nv = sv + 2;
    if (nk >= NSLOT) nk -= NSLOT;
    if (nv >= NSLOT) nv -= NSLOT;
    stage(T{}, T{}, T{}, X1{}, T{}, T{}, X2{}, NB_{}, sA, sB, pA, pB, sk * SLOT, sv * SLOT, sk * SLOT, sk * SLOT + 8192, sv * SLOT + 8192,
          sv * SLOT, 2 * t + 4, false);
    stage(T{}, T{}, T{}, X2{}, T{}, T{}, X1{}, X1{}, sB, sA, pB, pA, sk * SLOT + 8192, sv * SLOT + 8192, sv * SLOT, nk * SLOT, nv * SLOT,
          nk * SLOT, t < 5 ? 2 * t + 6 : -1, false);
    sk = nk; sv = nv;
  }
  stage(T{}, T{}, T{}, X1{}, T{}, T{}, X2{}, NB_{}, sA, sB, pA, pB, 2 * SLOT, SLOT, 2 * SLOT, 2 * SLOT + 8192, SLOT + 8192, SLOT, -1, false);
  stage(T{}, T{}, T{}, X2{}, F{}, T{}, X4{}, X1{}, sB, sA, pB, pA, 2 * SLOT + 8192, SLOT + 8192, SLOT, 0, 3 * SLOT, 3 * SLOT, -1, true);
  stage(F{}, T{}, T{}, X4{}, F{}, T{}, X0{}, NB_{}, sA, sB, pA, pB, 0, 3 * SLOT, 3 * SLOT, 0, 3 * SLOT + 8192, 0, -1, false);
  stage(F{}, T{}, F{}, X0{}, F{}, F{}, X0{}, NB_{}, sB, sA, pB, pA, 0, 3 * SLOT + 8192, 0, 0, 0, 0, -1, false);

  STAMP(4);
  char* og = (char*)O + (size_t)((p * NB + b) * NH + h) * (SQ * DH * 2);
  {
    const float rl = 1.f / x32_sum(l);
    char* orow = og + (32 * w + r32) * (DH * 2) + hh * 16;
#pragma unroll
    for (int db = 0; db < 4; ++db)
#pragma unroll
      for (int k = 0; k < 4; k += 2) {
        const unsigned a0 = pk2(o[db][4 * k] * rl, o[db][4 * k + 1] * rl), a1 = pk2(o[db][4 * k + 2] * rl, o[db][4 * k + 3] * rl);
        const unsigned b0 = pk2(o[db][4 * k + 4] * rl, o[db][4 * k + 5] * rl), b1 = pk2(o[db][4 * k + 6] * rl, o[db][4 * k + 7] * rl);
        auto r0 = __builtin_amdgcn_permlane32_swap(a0, b0, false, false);
        auto r1 = __builtin_amdgcn_permlane32_swap(a1, b1, false, false);
        *(u32x4*)(orow + db * 64 + k * 16) = (u32x4){r0[0], r1[0], r0[1], r1[1]};
      }
  }
  lx = xg_sum(lx);
  wait_bar<63>();
  {
    const unsigned pb = lds0 + w * 8192 + lane * 16;
#pragma unroll
    for (int dt = 0; dt < 8; ++dt) *(LDS_AS f32x4*)(pb + dt * 1024) = ox[dt];
    if (g == 0) { *(LDS_AS f32x2*)(lds0 + 32768 + w * 128 + c * 8) = (f32x2){mx, lx}; }
  }
  wait_bar<63>();
  {
    f32x2 ml[4];
#pragma unroll
    for (int i = 0; i < 4; ++i) ml[i] = *(const LDS_AS f32x2*)(lds0 + 32768 + i * 128 + c * 8);
    const float M = fmaxf(fmaxf(ml[0][0], ml[1][0]), fmaxf(ml[2][0], ml[3][0]));
    float sc[4], L = 0.f;
#pragma unroll
    for (int i = 0; i < 4; ++i) { sc[i] = ex2((ml[i][0] - M) * CEXP); L += sc[i] * ml[i][1]; }
    const float rl = 1.f / L;
    f32x4 a0 = f32x4{}, a1 = f32x4{};
#pragma unroll
    for (int i = 0; i < 4; ++i) {
      const unsigned pb = lds0 + i * 8192 + (2 * w) * 1024 + lane * 16;
      a0 += *(const LDS_AS f32x4*)(pb) * sc[i];
      a1 += *(const LDS_AS f32x4*)(pb + 1024) * sc[i];
    }
    *(u32x4*)(og + (128 + c) * (DH * 2) + w * 64 + g * 16) = c2frag(pk4(a0 * rl), pk4(a1 * rl));
  }
  STAMP(5); STAMP_FLUSH(0);
}

__global__ __launch_bounds__(512, 2) void mha_oproj(const u16* __restrict__ O, const u16* __restrict__ woA, const u16* __restrict__ woB,
                                                    float* __restrict__ slab) {
  extern __shared__ __attribute__((aligned(128))) char smem[];
  const int lane = threadIdx.x & 63;
  const int w8 = __builtin_amdgcn_readfirstlane(threadIdx.x >> 6), w = w8 & 3, hf = w8 >> 2;
  const unsigned ldsb = (unsigned)(uintptr_t)(LDS_AS char*)smem, lds0 = ldsb + hf * LDS_BYTES;
  const int g = lane >> 4, c = lane & 15;
  const int bid = blockIdx.x, p = (bid >> 2) & 1, kq = bid & 3, nt = (bid >> 3) & 3, b = bid >> 5;
  STAMP_DECL; STAMP(0);
  const int ksl = kq * 2 + hf;
  const char* ag = (const char*)O + (size_t)((p * NB + b) * NH + ksl * 4) * (SQ * DH * 2);
  const char* wg = (const char*)(p ? woB : woA) + (size_t)(ksl * 512) * (DM * 2) + nt * 256;
  f32x4 acc[9][2];
#pragma unroll
  for (int i = 0; i < 9; ++i) { acc[i][0] = f32x4{}; acc[i][1] = f32x4{}; }
  gemm144<DH * 2, SQ * DH * 2, DM * 2, false, WO_NT, O_NT>(acc, ag, wg, lds0, w, lane, ROT2, [](int, auto) {} STAMP_PASS);
  STAMP(1);
  wait_bar<63>();
  const unsigned xb = ldsb + w * 18432 + lane * 16;
  if (hf == 0) {
#pragma unroll
    for (int rt = 5; rt < 9; ++rt) { *(LDS_AS f32x4*)(xb + rt * 2048) = acc[rt][0]; *(LDS_AS f32x4*)(xb + rt * 2048 + 1024) = acc[rt][1]; }
  } else {
#pragma unroll
    for (int rt = 0; rt < 5; ++rt) { *(LDS_AS f32x4*)(xb + rt * 2048) = acc[rt][0]; *(LDS_AS f32x4*)(xb + rt * 2048 + 1024) = acc[rt][1]; }
  }
  wait_bar<63>();
  float* sg = slab + ((size_t)(kq * 2 + p) * (NB * SQ) + b * SQ + c) * DM + nt * 128 + 32 * w + g * 4;
  if (hf == 0) {
#pragma unroll
    for (int rt = 0; rt < 5; ++rt) {
      *(f32x4*)(sg + (size_t)(16 * rt) * DM) = acc[rt][0] + *(const LDS_AS f32x4*)(xb + rt * 2048);
      *(f32x4*)(sg + (size_t)(16 * rt) * DM + 16) = acc[rt][1] + *(const LDS_AS f32x4*)(xb + rt * 2048 + 1024);
    }
  } else {
#pragma unroll
    for (int rt = 5; rt < 9; ++rt) {
      *(f32x4*)(sg + (size_t)(16 * rt) * DM) = acc[rt][0] + *(const LDS_AS f32x4*)(xb + rt * 2048);
      *(f32x4*)(sg + (size_t)(16 * rt) * DM + 16) = acc[rt][1] + *(const LDS_AS f32x4*)(xb + rt * 2048 + 1024);
    }
  }
  STAMP(2); STAMP_FLUSH(1);
}

__global__ __launch_bounds__(256) void mha_reduce(const float* __restrict__ slab, u16* __restrict__ out) {
  const int i = blockIdx.x * 256 + threadIdx.x;
  const int e = i * 8, p = e / (NB * SQ * DM), rem = e % (NB * SQ * DM), row = rem / DM, n = rem % DM;
  f32x4 a = f32x4{}, b2 = f32x4{};
#pragma unroll
  for (int k = 0; k < 4; ++k) {
    const float* s = slab + (size_t)k * (2 * NB * SQ * DM) + e;
    a += *(const f32x4*)s; b2 += *(const f32x4*)(s + 4);
  }
  const u32x2 lo = pk4(a), hi = pk4(b2);
  *(u32x4*)(out + (size_t)row * (2 * DM) + p * DM + n) = (u32x4){lo[0], lo[1], hi[0], hi[1]};
}

extern "C" void kernel_launch(void* const* d_in, const int* in_sizes, int n_in, void* d_out, int out_size, void* d_ws, size_t ws_size,
                              hipStream_t stream) {
  (void)in_sizes; (void)n_in; (void)out_size; (void)ws_size;
  const u16* const* in = (const u16* const*)d_in;
  u16* O = (u16*)d_ws;
  float* slab = (float*)((char*)d_ws + (size_t)2 * NB * SQ * HD * 2);
  (void)hipFuncSetAttribute((const void*)mha_attn, hipFuncAttributeMaxDynamicSharedMemorySize, LDS_BYTES);
  (void)hipFuncSetAttribute((const void*)mha_oproj, hipFuncAttributeMaxDynamicSharedMemorySize, 2 * LDS_BYTES);
  mha_attn<<<GRID1, 256, LDS_BYTES, stream>>>(in[0], in[1], in[2], in[3], in[5], in[6], in[7], in[8], O);
  mha_oproj<<<256, 512, 2 * LDS_BYTES, stream>>>(O, in[4], in[9], slab);
  mha_reduce<<<(2 * NB * SQ * DM / 8) / 256, 256, 0, stream>>>(slab, (u16*)d_out);
}
```

```cpp
#include <hip/hip_runtime.h>
#include <hip/hip_bf16.h>
#include <stdint.h>
#include <type_traits>

typedef unsigned short u16;
typedef __attribute__((ext_vector_type(8))) short s16x8;
typedef __attribute__((ext_vector_type(4))) short s16x4;
typedef __attribute__((ext_vector_type(16))) float f32x16;
typedef __attribute__((ext_vector_type(4))) float f32x4;
typedef __attribute__((ext_vector_type(2))) float f32x2;
typedef __attribute__((ext_vector_type(4))) unsigned u32x4;
typedef __attribute__((ext_vector_type(2))) unsigned u32x2;
typedef __attribute__((ext_vector_type(2))) __bf16 bf16x2_t;
#define LDS_AS __attribute__((address_space(3)))
#define DI __device__ __forceinline__
#define SB() __builtin_amdgcn_sched_barrier(0)

constexpr int NB = 8, SQ = 144, DM = 512, NH = 32, SK = 512, DH = 128, HD = NH * DH;
constexpr int SLOT = 16384, NSLOT = 5, LDS_BYTES = SLOT * NSLOT;
constexpr unsigned WS_SLAB = 2u * NB * NH * SQ * DH * 2, WS_FLAG = WS_SLAB + 64u * 8 * 73728;
constexpr float CEXP = 0.08838834764831845f * 1.4426950408889634f;
constexpr float PSUM_LIMIT = 4096.f;

#define STAMP_DECL
#define STAMP_ARG
#define STAMP_PASS
#define STAMP(I) do {} while (0)
#define STAMP_ACC_BEGIN
#define STAMP_ACC_END(I) do {} while (0)
#define STAMP_FLUSH(K) do {} while (0)

#define W_NT false
#define WO_NT false
#define O_NT false
#define ROT1 (b + h)
#define ROT2 (b + 2 * nt)
#define GRID1 512
#define LD_SLOT 0
#define PRIO 1
#define PRE_ON 0
#define ACQ_ON true
#define X_NT false
template <int N, class F> DI void static_for(F&& f) {
  if constexpr (N > 0) { static_for<N - 1>(f); f(std::integral_constant<int, N - 1>{}); }
}

DI f32x16 mfmaL(s16x8 a, s16x8 b, f32x16 c) { return __builtin_amdgcn_mfma_f32_32x32x16_bf16(a, b, c, 0, 0, 0); }
DI f32x4 mfma32(s16x8 a, s16x8 b, f32x4 c) { return __builtin_amdgcn_mfma_f32_16x16x32_bf16(a, b, c, 0, 0, 0); }
DI f32x4 mfma16(s16x4 a, s16x4 b, f32x4 c) { return __builtin_amdgcn_mfma_f32_16x16x16bf16_1k(a, b, c, 0, 0, 0); }
DI s16x8 lds_b128(unsigned a) { return *(const LDS_AS s16x8*)(a); }
DI s16x4 lds_tr(unsigned a) { return __builtin_amdgcn_ds_read_tr16_b64_v4i16((LDS_AS s16x4*)(a)); }
DI s16x8 cat8(s16x4 lo, s16x4 hi) { return __builtin_shufflevector(lo, hi, 0, 1, 2, 3, 4, 5, 6, 7); }
DI unsigned pk2(float a, float b) { f32x2 v = {a, b}; return __builtin_bit_cast(unsigned, __builtin_convertvector(v, bf16x2_t)); }
DI u32x2 pk4(f32x4 v) { u32x2 r = {pk2(v[0], v[1]), pk2(v[2], v[3])}; return r; }
DI float ex2(float x) { return __builtin_amdgcn_exp2f(x); }

DI u32x4 c2frag(u32x2 t0, u32x2 t1) {
  u32x4 r;
#pragma unroll
  for (int i = 0; i < 2; ++i) {
    auto a = __builtin_amdgcn_permlane32_swap(t0[i], t1[i], false, false);
    auto b = __builtin_amdgcn_permlane16_swap(a[0], a[1], false, false);
    r[i] = b[0]; r[2 + i] = b[1];
  }
  return r;
}
DI float x32_max(float x) {
  auto b = __builtin_amdgcn_permlane32_swap(__float_as_uint(x), __float_as_uint(x), false, false);
  return fmaxf(__uint_as_float(b[0]), __uint_as_float(b[1]));
}
DI float x32_sum(float x) {
  auto b = __builtin_amdgcn_permlane32_swap(__float_as_uint(x), __float_as_uint(x), false, false);
  return __uint_as_float(b[0]) + __uint_as_float(b[1]);
}
DI float xg_max(float x) {
  auto a = __builtin_amdgcn_permlane16_swap(__float_as_uint(x), __float_as_uint(x), false, false);
  return x32_max(fmaxf(__uint_as_float(a[0]), __uint_as_float(a[1])));
}
DI float xg_sum(float x) {
  auto a = __builtin_amdgcn_permlane16_swap(__float_as_uint(x), __float_as_uint(x), false, false);
  return x32_sum(__uint_as_float(a[0]) + __uint_as_float(a[1]));
}
DI float max16(const f32x16& s) {
  float a = fmaxf(fmaxf(s[0], s[1]), s[2]), b = fmaxf(fmaxf(s[3], s[4]), s[5]);
  a = fmaxf(fmaxf(a, s[6]), s[7]); b = fmaxf(fmaxf(b, s[8]), s[9]);
  a = fmaxf(fmaxf(a, s[10]), s[11]); b = fmaxf(fmaxf(b, s[12]), s[13]);
  return fmaxf(fmaxf(a, s[14]), fmaxf(b, s[15]));
}

template <int GOFF, int LOFF, int POL>
DI void dma(unsigned voff, const char* sb, unsigned lb) {
  if constexpr (POL == 1)
    asm volatile("s_add_u32 m0, %2, %3\n\ts_nop 0\n\tglobal_load_lds_dwordx4 %0, %1 offset:%4 nt"
                 ::"v"(voff), "s"(sb), "s"(lb), "i"(LOFF - GOFF), "i"(GOFF) : "memory", "scc", "m0");
  else if constexpr (POL == 2)
    asm volatile("s_add_u32 m0, %2, %3\n\ts_nop 0\n\tglobal_load_lds_dwordx4 %0, %1 offset:%4 sc1"
                 ::"v"(voff), "s"(sb), "s"(lb), "i"(LOFF - GOFF), "i"(GOFF) : "memory", "scc", "m0");
  else
    asm volatile("s_add_u32 m0, %2, %3\n\ts_nop 0\n\tglobal_load_lds_dwordx4 %0, %1 offset:%4"
                 ::"v"(voff), "s"(sb), "s"(lb), "i"(LOFF - GOFF), "i"(GOFF) : "memory", "scc", "m0");
}
typedef unsigned long long u64;
typedef __attribute__((address_space(1))) u64 gu64;
extern "C" __device__ u64 hw_dispatch_id() __asm("llvm.amdgcn.dispatch.id");
DI void publish(u64* flag, u64 tag) {
  asm volatile("s_waitcnt vmcnt(0) lgkmcnt(0)\n\ts_barrier" ::: "memory");
  if (threadIdx.x == 0) __hip_atomic_store((gu64*)flag, tag, __ATOMIC_RELAXED, __HIP_MEMORY_SCOPE_AGENT);
}
DI void await(const u64* flags, int stride, int n, u64 tag, int w, int lane, bool acquire) {
  if (w == 0) {
    const gu64* f = (const gu64*)flags + (lane < n ? lane : 0) * stride;
    for (int spins = 0; spins < (1 << 20); ++spins) {
      if (__all(__hip_atomic_load(f, __ATOMIC_RELAXED, __HIP_MEMORY_SCOPE_AGENT) == tag)) break;
      __builtin_amdgcn_s_sleep(2);
    }
    if (acquire) __builtin_amdgcn_fence(__ATOMIC_ACQUIRE, "agent");
  }
  asm volatile("s_waitcnt vmcnt(0) lgkmcnt(0)\n\ts_barrier" ::: "memory");
}
template <int OFF> DI void gld_sc1(u32x4& d, unsigned voff, const char* sb) {
  asm volatile("global_load_dwordx4 %0, %1, %2 offset:%3 sc1" : "=v"(d) : "v"(voff), "s"(sb), "i"(OFF));
}
DI void gld_flag(u32x2& d, unsigned voff, const char* sb) {
  asm volatile("global_load_dwordx2 %0, %1, %2 sc1" : "=v"(d) : "v"(voff), "s"(sb));
}
template <int N> DI void wait_bar() { asm volatile("s_waitcnt vmcnt(%0) lgkmcnt(0)\n\ts_barrier" ::"i"(N) : "memory"); }
template <int N> DI void wait_vm() { asm volatile("s_waitcnt vmcnt(%0)" ::"i"(N) : "memory"); }

DI int tsw(int r) { return (r & 2) | ((r >> 2) & 1); }
constexpr int XBUF = 18432, WBUF = 4 * SLOT;

template <int AST, int ABLK, int WST, int WBLK, int XPOL>
struct Gemm {
  unsigned va[5], vw[4], lw, lx, lx4, bx0, bx1, bw0, bw1;
  const char *ag, *wsrc;
  int rot;
  DI Gemm(const char* ag_, const char* wsrc_, unsigned lds0, int w, int lane, int rot_) : ag(ag_), wsrc(wsrc_), rot(rot_) {
    const int g = lane >> 4, c = lane & 15, pos = (lane >> 2) & 7;
    const unsigned va0 = ((lane >> 3) * AST + (((lane & 7) ^ (lane >> 4)) << 4)) ^ ((w & 1) << 6);
    const unsigned vw0 = (4 * (lane >> 5) + (pos & 3) + 8 * (pos >> 2)) * WST + (((lane & 3) ^ ((pos >> 2) << 1)) << 4);
#pragma unroll
    for (int i = 0; i < 4; ++i) { va[i] = va0 + (8 * w + 32 * i) * AST; vw[i] = vw0 + w * 64 + 16 * i * WST; }
    va[4] = va0 + (128 + 8 * (w & 1)) * AST;
    lw = lds0 + WBUF + w * 4096; lx = lds0 + w * 1024; lx4 = lds0 + 16384 + (w & 1) * 1024;
    bx0 = lds0 + (c >> 3) * 1024 + (c & 7) * 128 + ((g ^ (c >> 1)) << 4); bx1 = bx0 ^ 64;
    bw0 = lw + (g >> 1) * 1024 + pos * 64 + ((pos >> 2) << 5) + (lane & 3) * 8; bw1 = bw0 ^ 32;
  }
  DI const char* wbase(int t) const { const int tr = (t + rot) & 7; return wsrc + (size_t)((tr >> 1) * WBLK + (tr & 1) * 64) * WST; }
  DI const char* xbase(int t) const { const int tr = (t + rot) & 7; return ag + (tr >> 1) * ABLK + (tr & 1) * 128; }
  template <int I> DI void issue_w(const char* sb) const { dma<0, I * 1024, 0>(vw[I], sb, lw); }
  template <int T, int I> DI void issue_x(const char* sb) const {
    if constexpr (I < 4) dma<0, (T % 3) * XBUF + I * 4096, XPOL>(va[I], sb, lx);
    else dma<0, (T % 3) * XBUF, XPOL>(va[4], sb, lx4);
  }
  DI void issue_w4(int t) const { const char* sb = wbase(t); issue_w<0>(sb); issue_w<1>(sb); issue_w<2>(sb); issue_w<3>(sb); }
  template <int T> DI void issue_x5() const {
    const char* sb = xbase(T);
    issue_x<T, 0>(sb); issue_x<T, 1>(sb); issue_x<T, 2>(sb); issue_x<T, 3>(sb); issue_x<T, 4>(sb);
  }
  DI void read_w(s16x8 (&f)[2][2]) const {
#pragma unroll
    for (int ks = 0; ks < 2; ++ks) {
      f[ks][0] = cat8(lds_tr(bw0 + ks * 2048), lds_tr(bw0 + ks * 2048 + 512));
      f[ks][1] = cat8(lds_tr(bw1 + ks * 2048), lds_tr(bw1 + ks * 2048 + 512));
    }
  }
  DI void read_w_now(s16x8 (&f)[2][2]) const {
    read_w(f);
    asm volatile("s_waitcnt lgkmcnt(0)" : "+v"(f[0][0]), "+v"(f[0][1]), "+v"(f[1][0]), "+v"(f[1][1])::"memory");
  }
  DI void w_free() const { asm volatile("s_waitcnt lgkmcnt(3)" ::: "memory"); }
  template <int S, class Hook> DI void step(f32x4 (&acc)[9][2], const s16x8 (&wf)[2][2], Hook&& hook) const {
    constexpr int D = 3, xo = (S % 3) * XBUF;
    s16x8 xf[18];
    auto ldx = [&](auto I) {
      constexpr int i = decltype(I)::value;
      if constexpr (i < 18) xf[i] = lds_b128((i >= 9 ? bx1 : bx0) + xo + (i % 9) * 2048);
    };
    static_for<D>(ldx);
    SB();
    static_for<18>([&](auto I) {
      constexpr int i = decltype(I)::value;
      hook(I);
      ldx(std::integral_constant<int, i + D>{});
      acc[i % 9][0] = mfma32(wf[i / 9][0], xf[i], acc[i % 9][0]);
      acc[i % 9][1] = mfma32(wf[i / 9][1], xf[i], acc[i % 9][1]);
      asm volatile("" : "+v"(acc[i % 9][0]), "+v"(acc[i % 9][1]));
      SB();
    });
  }
};

__global__ __launch_bounds__(256, 2) void mha_fused(const u16* __restrict__ xA, const u16* __restrict__ kA, const u16* __restrict__ vA,
                                                    const u16* __restrict__ wqA, const u16* __restrict__ woA, const u16* __restrict__ xB,
                                                    const u16* __restrict__ kB, const u16* __restrict__ vB, const u16* __restrict__ wqB,
                                                    const u16* __restrict__ woB, char* ws, u16* __restrict__ out) {
  extern __shared__ __attribute__((aligned(128))) char smem[];
  const unsigned lds0 = (unsigned)(uintptr_t)(LDS_AS char*)smem;
  const int lane = threadIdx.x & 63;
  const int w = __builtin_amdgcn_readfirstlane(threadIdx.x >> 6);
  const int g = lane >> 4, c = lane & 15, r32 = lane & 31, hh = lane >> 5;
  STAMP_DECL; STAMP(0);
  const int bid = blockIdx.x, p = (bid >> 2) & 1, sl = (bid >> 3) & 31;
  const int h = (bid & 3) * 8 + 2 * (sl & 3) + (bid >> 8), b = sl >> 2;
  const char* xg = (const char*)(p ? xB : xA) + (size_t)b * SQ * DM * 2;
  const char* wq = (const char*)(p ? wqB : wqA) + h * (DH * 2);
  const char* kg = (const char*)(p ? kB : kA) + (size_t)(b * NH + h) * SK * DH * 2 + w * 4096;
  const char* vg = (const char*)(p ? vB : vA) + (size_t)(b * NH + h) * SK * DH * 2 + w * 4096;
  const unsigned vkE = (lane >> 3) * 256 + (((lane & 7) ^ (lane >> 4)) << 4), vkO = vkE ^ 64;
  const unsigned vv = (lane >> 3) * 256 + (((lane & 7) ^ (tsw(lane >> 3) << 1)) << 4);
  const unsigned lkv = lds0 + w * 4096;
  auto issue_kv2 = [&](int j, unsigned so, auto I) {
    constexpr int i = decltype(I)::value;
    const char* s = ((j & 1) ? vg : kg) + (j >> 1) * SLOT;
    dma<(i & 1) * 128 + (i >> 1) * 2048, i * 1024, 1>((j & 1) ? vv : ((i >> 1) ? vkO : vkE), s, lkv + so);
  };
  auto issue_kv = [&](int j, auto I) { issue_kv2(j, ((j + 3) % NSLOT) * SLOT, I); };
  unsigned kb = lds0 + (r32 >> 3) * 2048 + (r32 & 7) * 128 + ((hh ^ ((r32 >> 1) & 7)) << 4);
  unsigned kbx = lds0 + w * 4096 + (c >> 3) * 2048 + (c & 7) * 128 + ((g ^ (c >> 1)) << 4);

  __builtin_amdgcn_s_setprio(1);
  s16x8 q[8], qx[4];
  {
    f32x4 acc[9][2];
#pragma unroll
    for (int i = 0; i < 9; ++i) { acc[i][0] = f32x4{}; acc[i][1] = f32x4{}; }
    const Gemm<DM * 2, 256, HD * 2, 128, 0> G(xg, wq, lds0, w, lane, b + h);
    s16x8 wf[2][2][2];
    G.issue_w4(0); G.issue_x5<0>(); G.issue_x5<1>();
    wait_vm<10>(); G.read_w_now(wf[0]);
    G.issue_w4(1);
    static_for<8>([&](auto S) {
      constexpr int s = decltype(S)::value;
      wait_bar<(s == 0) ? 9 : (s == 7) ? 12 : 13>();
      if constexpr (s == 0) STAMP(6);
      const char* sxb = G.xbase(s + 2);
      const char* swb = G.wbase(s + 2);
      G.template step<s>(acc, wf[s & 1], [&](auto I) {
        constexpr int i = decltype(I)::value;
        if constexpr (i < 9 && (i & 1) == 0) {
          if constexpr (s + 2 < 8) G.template issue_x<s + 2, (i >> 1)>(sxb);
          else if constexpr (i < 8) issue_kv(s == 6 ? 0 : 2, std::integral_constant<int, (i >> 1)>{});
        }
        if constexpr (i == 10 && s < 7) { wait_vm<(s == 6) ? 4 : 5>(); G.read_w(wf[(s + 1) & 1]); }
        if constexpr (i == 14 && s < 7) G.w_free();
        if constexpr (i >= 14 && s < 7) {
          if constexpr (s + 2 < 8) G.template issue_w<i - 14>(swb); else issue_kv(1, std::integral_constant<int, i - 14>{});
        }
      });
    });
    const unsigned qw = lds0 + SLOT + (c >> 3) * 2048 + (w >> 1) * 1024 + (c & 7) * 128 + (((4 * (w & 1) + g) ^ (c >> 1)) << 4);
    wait_bar<63>();
#pragma unroll
    for (int rt = 0; rt < 8; ++rt) *(LDS_AS u32x4*)(qw + rt * 4096) = c2frag(pk4(acc[rt][0]), pk4(acc[rt][1]));
    const u32x4 fx = c2frag(pk4(acc[8][0]), pk4(acc[8][1]));
    wait_bar<63>();
#pragma unroll
    for (int ks = 0; ks < 8; ++ks) q[ks] = lds_b128((kb ^ ((ks & 3) << 5)) + SLOT + w * 8192 + (ks >> 2) * 1024);
    asm volatile("s_waitcnt lgkmcnt(0)" : "+v"(q[0]), "+v"(q[1]), "+v"(q[2]), "+v"(q[3]), "+v"(q[4]), "+v"(q[5]), "+v"(q[6]), "+v"(q[7])::"memory");
    *(LDS_AS u32x4*)(lds0 + SLOT + w * 8192 + lane * 16) = fx;
    wait_bar<8>();
#pragma unroll
    for (int ks = 0; ks < 4; ++ks) qx[ks] = lds_b128(lds0 + SLOT + ks * 8192 + lane * 16);
  }

  f32x16 o[4];
  f32x4 ox[8];
#pragma unroll
  for (int i = 0; i < 4; ++i) o[i] = f32x16{};
#pragma unroll
  for (int i = 0; i < 8; ++i) ox[i] = f32x4{};
  float m, mx, l = 0.f, lx = 0.f;
  f32x16 sA, sB;
  f32x4 sx;
  u32x4 pA[2], pB[2];
  u32x2 px;
  const int vr = 4 * hh + ((lane >> 2) & 3);
  unsigned vb = lds0 + vr * 128 + (((g & 1) ^ tsw(vr)) << 5) + (lane & 3) * 8;
  const int vrx = (lane >> 2) & 7;
  unsigned vbx = lds0 + w * 4096 + (g >> 1) * 2048 + vrx * 128 + (tsw(vrx) << 5) + (lane & 3) * 8;

  s16x8 kf[4], vf[4], kx[4];
  s16x4 vx[4];
  auto ldk = [&](auto QK_, auto I, unsigned ko) {
    constexpr int i = decltype(I)::value;
    if constexpr (decltype(QK_)::value) kf[i & 3] = lds_b128(((kb + ko) ^ ((i & 3) << 5)) + (i >> 2) * 1024);
  };
  auto ldv = [&](auto PV_, auto I, unsigned vo) {
    constexpr int i = decltype(I)::value;
    if constexpr (decltype(PV_)::value) {
      const unsigned a = ((vb + vo) ^ (((i >> 1) & 1) << 6)) + (i & 1) * 4096 + (i >> 2) * 1024;
      vf[i & 3] = cat8(lds_tr(a), lds_tr(a + 2048));
    }
  };
  auto ldx = [&](auto XM_, auto I, unsigned xo) {
    constexpr int XM = decltype(XM_)::value, i = decltype(I)::value;
    if constexpr (XM == 1 && i < 4) kx[i] = lds_b128(((kbx + xo) ^ ((i & 1) << 6)) + (i >> 1) * 1024);
    if constexpr (XM == 2 || XM == 4) vx[i & 3] = lds_tr(((vbx + xo) ^ ((i & 3) << 5)) + (i >> 2) * 1024);
  };
  auto stage = [&](auto QK_, auto PV_, auto SM_, auto XM_, auto NQK_, auto NPV_, auto NXM_, auto BAR_, f32x16& sn, const f32x16& sc,
                   const u32x4 (&pc)[2], u32x4 (&pn)[2], unsigned ko, unsigned vo, unsigned xo, unsigned nko, unsigned nvo, unsigned nxo,
                   int jd, unsigned d0, unsigned d1, bool last) {
    constexpr bool QK = decltype(QK_)::value, PV = decltype(PV_)::value, SM = decltype(SM_)::value;
    constexpr int XM = decltype(XM_)::value, BAR = decltype(BAR_)::value;
    constexpr bool XPV = XM == 2 || XM == 4, XSM = XM == 2 || XM == 3;
    const float nm = -m * CEXP, nmx = -mx * CEXP;
    float ps = 0.f, psx = 0.f, e0 = 0.f, e1 = 0.f, x0 = 0.f, x1 = 0.f;
    u32x2 pxn;
    static_for<8>([&](auto I) {
      constexpr int i = decltype(I)::value;
      if constexpr (i == 6 && BAR >= 0) {
        STAMP_ACC_BEGIN;
        if constexpr (BAR == 0) { wait_bar<0>(); static_for<4>([&](auto J) { issue_kv(3, J); }); }
        else { if (last) wait_bar<0>(); else wait_bar<4>(); }
        STAMP_ACC_END(8);
      }
      if constexpr (QK) sn = mfmaL(kf[i & 3], q[i], i == 0 ? f32x16{} : sn);
      using I2 = std::integral_constant<int, (i + 2) & 7>;
      if constexpr (i < 6) ldk(QK_, I2{}, ko); else ldk(NQK_, I2{}, nko);
      float f0, f1;
      if constexpr (SM) {
        f0 = fmaf(sc[2 * i], CEXP, nm); f1 = fmaf(sc[2 * i + 1], CEXP, nm);
        if constexpr (i > 0) { ps += e0 + e1; pn[(i - 1) >> 2][(i - 1) & 3] = pk2(e0, e1); asm volatile("" : "+v"(pn[(i - 1) >> 2][(i - 1) & 3]), "+v"(ps)); }
        asm volatile("" : "+v"(f0), "+v"(f1));
      }
      SB();
      if constexpr (PV) o[i >> 1] = mfmaL(vf[i & 3], __builtin_bit_cast(s16x8, pc[i & 1]), o[i >> 1]);
      if constexpr (i < 6) ldv(PV_, I2{}, vo); else ldv(NPV_, I2{}, nvo);
      if constexpr (SM) { e0 = ex2(f0); e1 = ex2(f1); asm volatile("" : "+v"(e0), "+v"(e1)); }
      SB();
      if constexpr (XM == 1 && i < 4) sx = mfma32(kx[i], qx[i], i == 0 ? f32x4{} : sx);
      if constexpr (XPV) ox[i] = mfma16(vx[i & 3], __builtin_bit_cast(s16x4, px), ox[i]);
      if constexpr (i < 6) ldx(XM_, I2{}, xo); else ldx(NXM_, I2{}, nxo);
      if constexpr (XSM && (i == 2 || i == 4)) {
        x0 = ex2(fmaf(sx[i - 2], CEXP, nmx)); x1 = ex2(fmaf(sx[i - 1], CEXP, nmx));
        asm volatile("" : "+v"(x0), "+v"(x1));
      }
      if constexpr (XSM && (i == 3 || i == 5)) {
        psx += x0 + x1; pxn[(i - 3) >> 1] = pk2(x0, x1);
        asm volatile("" : "+v"(pxn[(i - 3) >> 1]), "+v"(psx));
      }
      if constexpr (BAR >= 0 && i >= 6) { if (jd >= 0) issue_kv2(jd, d0, std::integral_constant<int, i - 6>{}); }
      if constexpr (XM == 1 && PV && i < 6) {
        if (jd >= 0) issue_kv2(jd + ((i + 2) >> 2), i < 2 ? d0 : d1, std::integral_constant<int, ((i + 2) & 3)>{});
      }
      SB();
    });
    if constexpr (SM) {
      ps += e0 + e1; pn[1][3] = pk2(e0, e1);
      if (__any(!(ps <= PSUM_LIMIT))) {
        const float mn = fmaxf(m, x32_max(max16(sc)));
        const float al = ex2((m - mn) * CEXP), nn = -mn * CEXP;
        m = mn; l *= al; ps = 0.f;
#pragma unroll
        for (int i = 0; i < 4; ++i) o[i] *= al;
#pragma unroll
        for (int i = 0; i < 8; ++i) {
          const float a0 = ex2(fmaf(sc[2 * i], CEXP, nn)), a1 = ex2(fmaf(sc[2 * i + 1], CEXP, nn));
          ps += a0 + a1;
          pn[i >> 2][i & 3] = pk2(a0, a1);
        }
      }
      l += ps;
    }
    if constexpr (XSM) {
      if (__any(!(psx <= PSUM_LIMIT))) {
        const float mn = fmaxf(mx, xg_max(fmaxf(fmaxf(sx[0], sx[1]), fmaxf(sx[2], sx[3]))));
        const float al = ex2((mx - mn) * CEXP), nn = -mn * CEXP;
        mx = mn; lx *= al;
#pragma unroll
        for (int i = 0; i < 8; ++i) ox[i] *= al;
        const float a0 = ex2(fmaf(sx[0], CEXP, nn)), a1 = ex2(fmaf(sx[1], CEXP, nn)), a2 = ex2(fmaf(sx[2], CEXP, nn)),
                    a3 = ex2(fmaf(sx[3], CEXP, nn));
        psx = (a0 + a1) + (a2 + a3);
        pxn[0] = pk2(a0, a1); pxn[1] = pk2(a2, a3);
      }
      lx += psx;
      px = pxn;
    }
  };
  using T = std::true_type; using F = std::false_type;
  using X0 = std::integral_constant<int, 0>; using X1 = std::integral_constant<int, 1>; using X2 = std::integral_constant<int, 2>;
  using X3 = std::integral_constant<int, 3>; using X4 = std::integral_constant<int, 4>; using NB_ = std::integral_constant<int, -1>;

  STAMP(2);
  m = 0.f; mx = 0.f;
  ldk(T{}, X0{}, 3 * SLOT); ldk(T{}, X1{}, 3 * SLOT); ldx(X1{}, X0{}, 3 * SLOT); ldx(X1{}, X1{}, 3 * SLOT);
  SB();
  stage(T{}, F{}, F{}, X1{}, T{}, F{}, X3{}, NB_{}, sA, sB, pB, pA, 3 * SLOT, 0, 3 * SLOT, 3 * SLOT + 8192, 0, 0, -1, 0, 0, false);
  m = x32_max(max16(sA));
  mx = xg_max(fmaxf(fmaxf(sx[0], sx[1]), fmaxf(sx[2], sx[3])));
  stage(T{}, F{}, T{}, X3{}, T{}, T{}, X1{}, X0{}, sB, sA, pB, pA, 3 * SLOT + 8192, 0, 0, 0, 4 * SLOT, 0, 4, 2 * SLOT, 0, false);
  unsigned cv = 4 * SLOT, ck = 0, d0 = 2 * SLOT, d1 = 3 * SLOT;
  asm volatile("" : "+v"(kb), "+v"(kbx), "+v"(vb), "+v"(vbx));
  for (int t = 0; t < 6; ++t) {
    unsigned nk = ck + 2 * SLOT, nv = cv + 2 * SLOT;
    if (nk >= NSLOT * SLOT) nk -= NSLOT * SLOT;
    if (nv >= NSLOT * SLOT) nv -= NSLOT * SLOT;
    stage(T{}, T{}, T{}, X1{}, T{}, T{}, X2{}, NB_{}, sA, sB, pA, pB, ck, cv, ck, ck + 8192, cv + 8192, cv, 2 * t + 4, d0, d1, false);
    stage(T{}, T{}, T{}, X2{}, T{}, T{}, X1{}, X1{}, sB, sA, pB, pA, ck + 8192, cv + 8192, cv, nk, nv, nk, t < 5 ? 2 * t + 6 : -1, cv, 0, false);
    d0 = cv; d1 = ck; ck = nk; cv = nv;
  }
  const int ln = __builtin_amdgcn_mbcnt_hi(~0u, __builtin_amdgcn_mbcnt_lo(~0u, 0u));
  const int hq = bid & 3, nt = sl & 3, half = bid >> 8, h0 = 8 * hq + half;
  const char* wo = (const char*)(p ? woB : woA);
  const Gemm<DH * 2, 2 * SQ * DH * 2, DM * 2, 256, 2> G(ws + (size_t)((p * NB + b) * NH + h0) * (SQ * DH * 2),
                                                        wo + (size_t)(h0 * DH) * (DM * 2) + nt * 256, lds0, w, ln, 2 * nt);
  G.issue_w4(0);
  {
    const char* sb = wo + (size_t)(h * DH + b * 16 + w * 4) * (DM * 2);
    dma<0, 0, 0>(ln * 16, sb, lkv); dma<1024, 1024, 0>(ln * 16, sb, lkv); dma<2048, 2048, 0>(ln * 16, sb, lkv); dma<3072, 3072, 0>(ln * 16, sb, lkv);
  }
  asm volatile("" : "+v"(kb), "+v"(kbx), "+v"(vb), "+v"(vbx));
  stage(T{}, T{}, T{}, X1{}, T{}, T{}, X2{}, NB_{}, sA, sB, pA, pB, 2 * SLOT, SLOT, 2 * SLOT, 2 * SLOT + 8192, SLOT + 8192, SLOT, -1, 0, 0, false);
  stage(T{}, T{}, T{}, X2{}, F{}, T{}, X4{}, X1{}, sB, sA, pB, pA, 2 * SLOT + 8192, SLOT + 8192, SLOT, 0, 3 * SLOT, 3 * SLOT, -1, 0, 0, true);
  stage(F{}, T{}, T{}, X4{}, F{}, T{}, X0{}, NB_{}, sA, sB, pA, pB, 0, 3 * SLOT, 3 * SLOT, 0, 3 * SLOT + 8192, 0, -1, 0, 0, false);
  stage(F{}, T{}, F{}, X0{}, F{}, F{}, X0{}, NB_{}, sB, sA, pB, pA, 0, 3 * SLOT + 8192, 0, 0, 0, 0, -1, 0, 0, false);

  STAMP(4);
  const int g2 = ln >> 4, c2 = ln & 15;
  const u64 tag = (hw_dispatch_id() + 1) * 0x9E3779B97F4A7C15ull ^ (u64)(uintptr_t)__builtin_amdgcn_dispatch_ptr() * 0xC2B2AE3D27D4EB4Full;
  u64* flagO = (u64*)(ws + WS_FLAG);
  u64* flagP = flagO + 512 * 8;
  u64* flagS = flagP + 256 * 8;
  const auto rsO = __builtin_amdgcn_make_buffer_rsrc(ws, 0, WS_SLAB, 0x00020000);
  const auto rsS = __builtin_amdgcn_make_buffer_rsrc(ws + WS_SLAB, 0, WS_FLAG - WS_SLAB, 0x00020000);
  const unsigned ob = ((p * NB + b) * NH + h) * (SQ * DH * 2);
  s16x8 wf[2][2][2];
  lx = xg_sum(lx);
  wait_bar<0>();
  G.read_w_now(wf[0]);
  G.issue_w4(1);
  constexpr int SCR = 2 * XBUF;
  {
    const float rl = 1.f / x32_sum(l);
    const int r = ln & 31;
    const unsigned sw = lds0 + (4 * w + (r >> 3)) * 1024 + (r & 7) * 128;
#pragma unroll
    for (int db = 0; db < 4; ++db)
#pragma unroll
      for (int k = 0; k < 4; k += 2) {
        const unsigned a0 = pk2(o[db][4 * k] * rl, o[db][4 * k + 1] * rl), a1 = pk2(o[db][4 * k + 2] * rl, o[db][4 * k + 3] * rl);
        const unsigned b0 = pk2(o[db][4 * k + 4] * rl, o[db][4 * k + 5] * rl), b1 = pk2(o[db][4 * k + 6] * rl, o[db][4 * k + 7] * rl);
        auto r0 = __builtin_amdgcn_permlane32_swap(a0, b0, false, false);
        auto r1 = __builtin_amdgcn_permlane32_swap(a1, b1, false, false);
        *(LDS_AS u32x4*)(sw + (db >> 1) * XBUF + (((4 * (db & 1) + k + (ln >> 5)) ^ ((r >> 1) & 7)) << 4)) = (u32x4){r0[0], r1[0], r0[1], r1[1]};
      }
#pragma unroll
    for (int dt = 0; dt < 8; ++dt)
      if ((dt >> 1) != w) *(LDS_AS f32x4*)(lds0 + SCR + (((dt >> 1) * 3 + ((w - (dt >> 1) - 1) & 3)) * 2 + (dt & 1)) * 1024 + ln * 16) = ox[dt];
    if (g2 == 0) { *(LDS_AS f32x2*)(lds0 + SCR + 24576 + w * 128 + c2 * 8) = (f32x2){mx, lx}; }
#pragma unroll
    for (int bf = 0; bf < 2; ++bf)
#pragma unroll
      for (int n = 0; n < 4; ++n) {
        const u32x4 v = *(const LDS_AS u32x4*)(lds0 + bf * XBUF + (4 * w + n) * 1024 + ln * 16);
        __builtin_amdgcn_raw_buffer_store_b128(
            v, rsO, ob + (32 * w + 8 * n + (ln >> 3)) * 256 + bf * 128 + (((ln & 7) ^ ((4 * n + g2) & 7)) << 4), 0, 16);
      }
  }
  wait_bar<63>();
  {
    f32x2 ml[4];
#pragma unroll
    for (int i = 0; i < 4; ++i) ml[i] = *(const LDS_AS f32x2*)(lds0 + SCR + 24576 + i * 128 + c2 * 8);
    const float M = fmaxf(fmaxf(ml[0][0], ml[1][0]), fmaxf(ml[2][0], ml[3][0]));
    float L = 0.f;
#pragma unroll
    for (int i = 0; i < 4; ++i) L += ex2((ml[i][0] - M) * CEXP) * ml[i][1];
    const float rl = 1.f / L;
    f32x4 a0 = w == 0 ? ox[0] : w == 1 ? ox[2] : w == 2 ? ox[4] : ox[6], a1 = w == 0 ? ox[1] : w == 1 ? ox[3] : w == 2 ? ox[5] : ox[7];
    const float so = ex2((mx - M) * CEXP) * rl;
    a0 *= so; a1 *= so;
#pragma unroll
    for (int i = 0; i < 3; ++i) {
      const f32x2 mi = *(const LDS_AS f32x2*)(lds0 + SCR + 24576 + ((w + 1 + i) & 3) * 128 + c2 * 8);
      const float si = ex2((mi[0] - M) * CEXP) * rl;
      const unsigned pb = lds0 + SCR + (w * 3 + i) * 2048 + ln * 16;
      a0 += *(const LDS_AS f32x4*)(pb) * si;
      a1 += *(const LDS_AS f32x4*)(pb + 1024) * si;
    }
    const u32x4 v = c2frag(pk4(a0), pk4(a1));
    *(LDS_AS u32x4*)(lds0 + (w >> 1) * XBUF + (16 + (c2 >> 3)) * 1024 + (c2 & 7) * 128 + (((4 * (w & 1) + g2) ^ (c2 >> 1)) << 4)) = v;
    __builtin_amdgcn_raw_buffer_store_b128(v, rsO, ob + (128 + c2) * 256 + w * 64 + g2 * 16, 0, 16);
  }
  STAMP(5);
  if (half == 0) __builtin_amdgcn_s_setprio(0);
  f32x4 acc[9][2];
#pragma unroll
  for (int i = 0; i < 9; ++i) { acc[i][0] = f32x4{}; acc[i][1] = f32x4{}; }
  const unsigned foff = ln == 4 ? (512 + (bid & 255)) * 64 : (((p * NB + b) * NH + h0) + 2 * ((nt + ln) & 3)) * 64;
  u32x2 fl;
  auto fl_load = [&]() { gld_flag(fl, foff, (const char*)flagO); };
  auto fl_bit = [&](int k) {
    asm volatile("" : "+v"(fl));
    return (bool)((__ballot((((u64)fl[1] << 32) | fl[0]) == tag) >> k) & 1);
  };
  auto fl_spin = [&](int k) {
    const gu64* f = (const gu64*)((const char*)flagO + __builtin_amdgcn_readlane(foff, k));
    for (int spins = 0; spins < (1 << 17); ++spins) {
      if (__hip_atomic_load(f, __ATOMIC_RELAXED, __HIP_MEMORY_SCOPE_AGENT) == tag) break;
      __builtin_amdgcn_s_sleep(2);
    }
  };
  u32x4 pp[9][2];
  bool early = false;
  const unsigned pbo = (bid & 255) * 73728 + w * 2048 + ln * 16;
  auto pp_load = [&]() {
    static_for<9>([&](auto R) {
      constexpr int rt = decltype(R)::value;
      gld_sc1<0>(pp[rt][0], pbo + rt * 8192, ws + WS_SLAB); gld_sc1<1024>(pp[rt][1], pbo + rt * 8192, ws + WS_SLAB);
    });
  };
  static_for<8>([&](auto S) {
    constexpr int s = decltype(S)::value;
    wait_bar<(s == 0 || s == 7) ? 63 : (s == 1) ? 4 : (s & 1) ? 13 : 9>();
    if constexpr (s == 1) {
      if (threadIdx.x == 0)
        __hip_atomic_store((gu64*)(flagO + ((p * NB + b) * NH + h) * 8), tag, __ATOMIC_RELAXED, __HIP_MEMORY_SCOPE_AGENT);
    }
    if constexpr (s == 2) STAMP(7);
    if constexpr (s == 1 || s == 2 || s == 4) { constexpr int k = s == 1 ? 1 : s == 2 ? 2 : 3; if (!fl_bit(k)) fl_spin(k); }
    bool pready = false;
    if constexpr (s == 6) { pready = fl_bit(4); pready = pready && half; }
    if constexpr (s == 0 || s == 1 || s == 3 || s == 5) fl_load();
    const char* sxb = G.xbase(s == 1 ? 2 : s + 2);
    const char* sxb3 = G.xbase(3);
    const char* swb = G.wbase(s + 2);
    G.template step<s>(acc, wf[s & 1], [&](auto I) {
      constexpr int i = decltype(I)::value;
      if constexpr (s == 1 && i < 5) G.template issue_x<2, i>(sxb);
      if constexpr (s == 1 && i >= 5 && i < 10) G.template issue_x<3, i - 5>(sxb3);
      if constexpr (s >= 2 && s < 6 && i < 9 && (i & 1) == 0) G.template issue_x<s + 2, (i >> 1)>(sxb);
      if constexpr (i == 10 && s < 7) {
        wait_vm<(s == 0) ? 10 : (s == 1) ? 11 : (s == 6) ? 0 : (s & 1) ? 6 : 5>();
        G.read_w(wf[(s + 1) & 1]);
      }
      if constexpr (i == 11 && s == 6) { if (pready) { pp_load(); early = true; } }
      if constexpr (i == 14 && s < 6) G.w_free();
      if constexpr (i >= 14 && s < 6) G.template issue_w<i - 14>(swb);
    });
  });
  STAMP(3);
  if (half == 0) {
#pragma unroll
    for (int rt = 0; rt < 9; ++rt) {
      __builtin_amdgcn_raw_buffer_store_b128(__builtin_bit_cast(u32x4, acc[rt][0]), rsS, pbo + rt * 8192, 0, 16);
      __builtin_amdgcn_raw_buffer_store_b128(__builtin_bit_cast(u32x4, acc[rt][1]), rsS, pbo + rt * 8192 + 1024, 0, 16);
    }
    publish(flagP + (bid & 255) * 8, tag);
  } else {
    if (!early) { fl_spin(4); pp_load(); }
    asm volatile("s_waitcnt vmcnt(0)"
                 : "+v"(pp[0][0]), "+v"(pp[0][1]), "+v"(pp[1][0]), "+v"(pp[1][1]), "+v"(pp[2][0]), "+v"(pp[2][1]), "+v"(pp[3][0]), "+v"(pp[3][1]),
                   "+v"(pp[4][0]), "+v"(pp[4][1]), "+v"(pp[5][0]), "+v"(pp[5][1]), "+v"(pp[6][0]), "+v"(pp[6][1]), "+v"(pp[7][0]), "+v"(pp[7][1]),
                   "+v"(pp[8][0]), "+v"(pp[8][1]));
    const int tile = (p * NB + b) * 4 + nt;
    const int own0 = hq == 0 ? 0 : 2 * hq + 1, own1 = 2 * hq + 3;
    const unsigned sb = (256u + tile * 4) * 73728 + w * 2048 + ln * 16;
#pragma unroll
    for (int rt = 0; rt < 9; ++rt) {
      acc[rt][0] = __builtin_bit_cast(f32x4, pp[rt][0]) + acc[rt][0];
      acc[rt][1] = __builtin_bit_cast(f32x4, pp[rt][1]) + acc[rt][1];
      if (rt < own0 || rt >= own1) {
        __builtin_amdgcn_raw_buffer_store_b128(__builtin_bit_cast(u32x4, acc[rt][0]), rsS, sb + hq * 73728 + rt * 8192, 0, 16);
        __builtin_amdgcn_raw_buffer_store_b128(__builtin_bit_cast(u32x4, acc[rt][1]), rsS, sb + hq * 73728 + rt * 8192 + 1024, 0, 16);
      }
    }
    publish(flagS + (tile * 4 + hq) * 8, tag);
    await(flagS + tile * 32, 8, 4, tag, w, ln, false);
    u16* orow = out + (size_t)(b * SQ + c2) * (2 * DM) + p * DM + nt * 128 + 32 * w + 8 * g2;
#pragma unroll
    for (int rt = 0; rt < 9; ++rt)
      if (rt >= own0 && rt < own1) {
        f32x4 t0[4], t1[4];
#pragma unroll
        for (int k = 0; k < 4; ++k) {
          const unsigned a = sb + (k == hq ? 0x7f000000u : k * 73728 + rt * 8192);
          t0[k] = __builtin_bit_cast(f32x4, __builtin_amdgcn_raw_buffer_load_b128(rsS, a, 0, 16));
          t1[k] = __builtin_bit_cast(f32x4, __builtin_amdgcn_raw_buffer_load_b128(rsS, a + 1024, 0, 16));
        }
        f32x4 s0 = f32x4{}, s1 = f32x4{};
#pragma unroll
        for (int k = 0; k < 4; ++k) {
          s0 += k == hq ? acc[rt][0] : t0[k];
          s1 += k == hq ? acc[rt][1] : t1[k];
        }
        *(u32x4*)(orow + (size_t)(16 * rt) * (2 * DM)) = c2frag(pk4(s0), pk4(s1));
      }
  }
  STAMP(1); STAMP_FLUSH(0);
}

extern "C" void kernel_launch(void* const* d_in, const int* in_sizes, int n_in, void* d_out, int out_size, void* d_ws, size_t ws_size,
                              hipStream_t stream) {
  (void)in_sizes; (void)n_in; (void)out_size; (void)ws_size;
  const u16* const* in = (const u16* const*)d_in;
  (void)hipFuncSetAttribute((const void*)mha_fused, hipFuncAttributeMaxDynamicSharedMemorySize, LDS_BYTES);
  mha_fused<<<512, 256, LDS_BYTES, stream>>>(in[0], in[1], in[2], in[3], in[4], in[5], in[6], in[7], in[8], in[9], (char*)d_ws,
                                             (u16*)d_out);
}
```

```cpp
#include <hip/hip_runtime.h>
#include <hip/hip_bf16.h>
#include <stdint.h>
#include <type_traits>

typedef unsigned short u16;
typedef __attribute__((ext_vector_type(8))) short s16x8;
typedef __attribute__((ext_vector_type(4))) short s16x4;
typedef __attribute__((ext_vector_type(16))) float f32x16;
typedef __attribute__((ext_vector_type(4))) float f32x4;
typedef __attribute__((ext_vector_type(2))) float f32x2;
typedef __attribute__((ext_vector_type(4))) unsigned u32x4;
typedef __attribute__((ext_vector_type(2))) unsigned u32x2;
typedef __attribute__((ext_vector_type(2))) __bf16 bf16x2_t;
#define LDS_AS __attribute__((address_space(3)))
#define DI __device__ __forceinline__
#define SB() __builtin_amdgcn_sched_barrier(0)

constexpr int NB = 8, SQ = 144, DM = 512, NH = 32, SK = 512, DH = 128, HD = NH * DH;
constexpr int SLOT = 16384, NSLOT = 5, LDS_BYTES = SLOT * NSLOT;
constexpr unsigned WS_SLAB = 2u * NB * NH * SQ * DH * 2, WS_FLAG = WS_SLAB + 64u * 4 * 73728;
constexpr float CEXP = 0.08838834764831845f * 1.4426950408889634f;
constexpr float PSUM_LIMIT = 4096.f;

#define STAMP_DECL
#define STAMP_ARG
#define STAMP_PASS
#define STAMP(I) do {} while (0)
#define STAMPR(I) do {} while (0)
#define STAMP_ACC_BEGIN
#define STAMP_ACC_END(I) do {} while (0)
#define STAMP_FLUSH(K) do {} while (0)

#define W_NT false
#define WO_NT false
#define O_NT false
#define ROT1 (b + h)
#define ROT2 (b + 2 * nt)
#define GRID1 512
#define LD_SLOT 0
#define PRIO 1
#define PRE_ON 0
#define ACQ_ON true
#define QX_POL 0
#define QW_POL 0
#define OW_POL 0
#define OX_POL 2
#define ST_AUX 16
#define X_NT false
template <int N, class F> DI void static_for(F&& f) {
  if constexpr (N > 0) { static_for<N - 1>(f); f(std::integral_constant<int, N - 1>{}); }
}

DI f32x16 mfmaL(s16x8 a, s16x8 b, f32x16 c) { return __builtin_amdgcn_mfma_f32_32x32x16_bf16(a, b, c, 0, 0, 0); }
DI f32x4 mfma32(s16x8 a, s16x8 b, f32x4 c) { return __builtin_amdgcn_mfma_f32_16x16x32_bf16(a, b, c, 0, 0, 0); }
DI f32x4 mfma16(s16x4 a, s16x4 b, f32x4 c) { return __builtin_amdgcn_mfma_f32_16x16x16bf16_1k(a, b, c, 0, 0, 0); }
DI s16x8 lds_b128(unsigned a) { return *(const LDS_AS s16x8*)(a); }
DI s16x4 lds_tr(unsigned a) { return __builtin_amdgcn_ds_read_tr16_b64_v4i16((LDS_AS s16x4*)(a)); }
DI s16x8 cat8(s16x4 lo, s16x4 hi) { return __builtin_shufflevector(lo, hi, 0, 1, 2, 3, 4, 5, 6, 7); }
DI unsigned pk2(float a, float b) { f32x2 v = {a, b}; return __builtin_bit_cast(unsigned, __builtin_convertvector(v, bf16x2_t)); }
DI u32x2 pk4(f32x4 v) { u32x2 r = {pk2(v[0], v[1]), pk2(v[2], v[3])}; return r; }
DI float ex2(float x) { return __builtin_amdgcn_exp2f(x); }

DI u32x4 c2frag(u32x2 t0, u32x2 t1) {
  u32x4 r;
#pragma unroll
  for (int i = 0; i < 2; ++i) {
    auto a = __builtin_amdgcn_permlane32_swap(t0[i], t1[i], false, false);
    auto b = __builtin_amdgcn_permlane16_swap(a[0], a[1], false, false);
    r[i] = b[0]; r[2 + i] = b[1];
  }
  return r;
}
DI float x32_max(float x) {
  auto b = __builtin_amdgcn_permlane32_swap(__float_as_uint(x), __float_as_uint(x), false, false);
  return fmaxf(__uint_as_float(b[0]), __uint_as_float(b[1]));
}
DI float x32_sum(float x) {
  auto b = __builtin_amdgcn_permlane32_swap(__float_as_uint(x), __float_as_uint(x), false, false);
  return __uint_as_float(b[0]) + __uint_as_float(b[1]);
}
DI float xg_max(float x) {
  auto a = __builtin_amdgcn_permlane16_swap(__float_as_uint(x), __float_as_uint(x), false, false);
  return x32_max(fmaxf(__uint_as_float(a[0]), __uint_as_float(a[1])));
}
DI float xg_sum(float x) {
  auto a = __builtin_amdgcn_permlane16_swap(__float_as_uint(x), __float_as_uint(x), false, false);
  return x32_sum(__uint_as_float(a[0]) + __uint_as_float(a[1]));
}
DI float max16(const f32x16& s) {
  float a = fmaxf(fmaxf(s[0], s[1]), s[2]), b = fmaxf(fmaxf(s[3], s[4]), s[5]);
  a = fmaxf(fmaxf(a, s[6]), s[7]); b = fmaxf(fmaxf(b, s[8]), s[9]);
  a = fmaxf(fmaxf(a, s[10]), s[11]); b = fmaxf(fmaxf(b, s[12]), s[13]);
  return fmaxf(fmaxf(a, s[14]), fmaxf(b, s[15]));
}

template <int GOFF, int LOFF, int POL>
DI void dma(unsigned voff, const char* sb, unsigned lb) {
  if constexpr (POL == 1)
    asm volatile("s_add_u32 m0, %2, %3\n\ts_nop 0\n\tglobal_load_lds_dwordx4 %0, %1 offset:%4 nt"
                 ::"v"(voff), "s"(sb), "s"(lb), "i"(LOFF - GOFF), "i"(GOFF) : "memory", "scc", "m0");
  else if constexpr (POL == 2)
    asm volatile("s_add_u32 m0, %2, %3\n\ts_nop 0\n\tglobal_load_lds_dwordx4 %0, %1 offset:%4 sc1"
                 ::"v"(voff), "s"(sb), "s"(lb), "i"(LOFF - GOFF), "i"(GOFF) : "memory", "scc", "m0");
  else
    asm volatile("s_add_u32 m0, %2, %3\n\ts_nop 0\n\tglobal_load_lds_dwordx4 %0, %1 offset:%4"
                 ::"v"(voff), "s"(sb), "s"(lb), "i"(LOFF - GOFF), "i"(GOFF) : "memory", "scc", "m0");
}
typedef unsigned long long u64;
typedef __attribute__((address_space(1))) u64 gu64;
extern "C" __device__ u64 hw_dispatch_id() __asm("llvm.amdgcn.dispatch.id");
DI void publish(u64* flag, u64 tag) {
  asm volatile("s_waitcnt vmcnt(0) lgkmcnt(0)\n\ts_barrier" ::: "memory");
  if (threadIdx.x == 0) __hip_atomic_store((gu64*)flag, tag, __ATOMIC_RELAXED, __HIP_MEMORY_SCOPE_AGENT);
}
DI void await(const u64* flags, int stride, int n, u64 tag, int w, int lane, bool acquire) {
  if (w == 0) {
    const gu64* f = (const gu64*)flags + (lane < n ? lane : 0) * stride;
    for (int spins = 0; spins < (1 << 20); ++spins) {
      if (__all(__hip_atomic_load(f, __ATOMIC_RELAXED, __HIP_MEMORY_SCOPE_AGENT) == tag)) break;
      __builtin_amdgcn_s_sleep(2);
    }
    if (acquire) __builtin_amdgcn_fence(__ATOMIC_ACQUIRE, "agent");
  }
  asm volatile("s_waitcnt vmcnt(0) lgkmcnt(0)\n\ts_barrier" ::: "memory");
}
template <int OFF> DI void gld_sc1(u32x4& d, unsigned voff, const char* sb) {
  asm volatile("global_load_dwordx4 %0, %1, %2 offset:%3 sc1" : "=v"(d) : "v"(voff), "s"(sb), "i"(OFF));
}
template <int OFF> DI void gld_nt(u32x4& d, unsigned voff, const char* sb) {
  asm volatile("global_load_dwordx4 %0, %1, %2 offset:%3 nt" : "=v"(d) : "v"(voff), "s"(sb), "i"(OFF));
}
DI void gld_flag(u32x2& d, unsigned voff, const char* sb) {
  asm volatile("global_load_dwordx2 %0, %1, %2 sc1" : "=v"(d) : "v"(voff), "s"(sb));
}
template <int N> DI void wait_bar() { asm volatile("s_waitcnt vmcnt(%0) lgkmcnt(0)\n\ts_barrier" ::"i"(N) : "memory"); }
template <int N> DI void wait_vm() { asm volatile("s_waitcnt vmcnt(%0)" ::"i"(N) : "memory"); }

DI int tsw(int r) { return (r & 2) | ((r >> 2) & 1); }
constexpr int XB[2] = {47104, 0}, WB[2] = {4 * SLOT, 18432}, SCR = 18432;

template <int AST, int ABLK, int WST, int WBLK, int XPOL, int WPOL>
struct Gemm {
  unsigned va[5], vw[2], lw, lx, lx4, bx0, bx1, bw0, bw1;
  const char *ag, *wsrc;
  int rot;
  bool x5;
  DI Gemm(const char* ag_, const char* wsrc_, unsigned lds0, unsigned ldsx, int w, int lane, int rot_)
      : ag(ag_), wsrc(wsrc_), rot(rot_) {
    const int g = lane >> 4, c = lane & 15, pos = lane >> 3, row = (lane >> 2) & 7;
    const unsigned va0 = ((lane >> 3) * AST + (((lane & 7) ^ (lane >> 4)) << 4)) ^ ((w & 1) << 6);
#pragma unroll
    for (int i = 0; i < 4; ++i) va[i] = va0 + (8 * w + 32 * i) * AST;
    va[4] = va0 + (128 + 8 * (w & 1)) * AST;
    vw[0] = (16 * w + (pos & 3) + 8 * (pos >> 2)) * WST + (((lane & 7) ^ (tsw(pos) << 1)) << 4); vw[1] = vw[0] + 4 * WST;
    x5 = w < 2;
    lw = lds0 + w * 4096; lx = ldsx + w * 1024; lx4 = ldsx + 16384 + (w & 1) * 1024;
    bx0 = ldsx + (c >> 3) * 1024 + (c & 7) * 128 + ((g ^ (c >> 1)) << 4); bx1 = bx0 ^ 64;
    bw0 = lds0 + (g >> 1) * 4096 + (w >> 1) * 1024 + row * 128 + (((2 * (w & 1)) ^ tsw(row)) << 5) + (lane & 3) * 8; bw1 = bw0 ^ 32;
  }
  DI const char* wbase(int t) const { const int tr = (t + rot) & 7; return wsrc + (size_t)((tr >> 1) * WBLK + (tr & 1) * 64) * WST; }
  DI const char* xbase(int t) const { const int tr = (t + rot) & 7; return ag + (tr >> 1) * ABLK + (tr & 1) * 128; }
  template <int T, int I> DI void issue_w(const char* sb) const { dma<(I & 1) * 128, WB[T & 1] + I * 1024, WPOL>(vw[I >> 1], sb, lw); }
  template <int T, int I> DI void issue_w_other(const char* sb) const {
    dma<256 + (I & 1) * 128, LDS_BYTES + WB[T & 1] + I * 1024, WPOL>(vw[I >> 1], sb, lw);
  }
  template <int T, int I> DI void issue_x(const char* sb) const {
    if constexpr (I < 4) dma<0, XB[T & 1] + I * 4096, XPOL>(va[I], sb, lx);
    else { if (x5) dma<0, XB[T & 1], XPOL>(va[4], sb, lx4); }
  }
  template <int T> DI void issue_w4() const {
    const char* sb = wbase(T);
    issue_w<T, 0>(sb); issue_w<T, 1>(sb); issue_w<T, 2>(sb); issue_w<T, 3>(sb);
  }
  template <int T> DI void issue_x5() const {
    const char* sb = xbase(T);
    issue_x<T, 0>(sb); issue_x<T, 1>(sb); issue_x<T, 2>(sb); issue_x<T, 3>(sb); issue_x<T, 4>(sb);
  }
  template <int T, int N> DI void issue(const char* swb, const char* sxb) const {
    if constexpr (N < 4) issue_w<T, N>(swb); else issue_x<T, N - 4>(sxb);
  }
  template <class Hook> DI void run(f32x4 (&acc)[9][2], Hook&& hook) const {
    s16x8 xf[4], wf[2][2][2];
    auto ldx = [&](auto N) {
      constexpr int n = decltype(N)::value, s = n / 18, i = n % 18;
      if constexpr (n < 144) xf[n & 3] = lds_b128((i >= 9 ? bx1 : bx0) + XB[s & 1] + (i % 9) * 2048);
    };
    auto ldw = [&](auto S, auto KS) {
      constexpr int s = decltype(S)::value, ks = decltype(KS)::value, wo = WB[s & 1] + ks * 8192;
      wf[s & 1][ks][0] = cat8(lds_tr(bw0 + wo), lds_tr(bw0 + wo + 2048));
      wf[s & 1][ks][1] = cat8(lds_tr(bw1 + wo), lds_tr(bw1 + wo + 2048));
    };
    using Z = std::integral_constant<int, 0>; using O1 = std::integral_constant<int, 1>;
    ldw(Z{}, Z{});
    static_for<3>(ldx);
    SB();
    static_for<144>([&](auto N) {
      constexpr int n = decltype(N)::value, s = n / 18, i = n % 18;
      hook(std::integral_constant<int, s>{}, std::integral_constant<int, i>{});
      if constexpr (i == 15 && s < 7) ldw(std::integral_constant<int, s + 1>{}, Z{});
      if constexpr (i == 3) ldw(std::integral_constant<int, s>{}, O1{});
      ldx(std::integral_constant<int, n + 3>{});
      acc[i % 9][0] = mfma32(wf[s & 1][i / 9][0], xf[n & 3], acc[i % 9][0]);
      acc[i % 9][1] = mfma32(wf[s & 1][i / 9][1], xf[n & 3], acc[i % 9][1]);
      asm volatile("" : "+v"(acc[i % 9][0]), "+v"(acc[i % 9][1]));
      SB();
    });
  }
};

__global__ __launch_bounds__(512, 2) void mha_fused(const u16* __restrict__ xA, const u16* __restrict__ kA, const u16* __restrict__ vA,
                                                    const u16* __restrict__ wqA, const u16* __restrict__ woA, const u16* __restrict__ xB,
                                                    const u16* __restrict__ kB, const u16* __restrict__ vB, const u16* __restrict__ wqB,
                                                    const u16* __restrict__ woB, char* ws, u16* out) {
  extern __shared__ __attribute__((aligned(128))) char smem[];
  const int lane = threadIdx.x & 63;
  const int w8 = __builtin_amdgcn_readfirstlane(threadIdx.x >> 6), w = w8 & 3, half = w8 >> 2;
  const unsigned ldsb = (unsigned)(uintptr_t)(LDS_AS char*)smem, lds0 = ldsb + half * LDS_BYTES;
  const int g = lane >> 4, c = lane & 15, r32 = lane & 31, hh = lane >> 5;
  STAMP_DECL; STAMP(0);
  const int bid = blockIdx.x, p = (bid >> 2) & 1, hq = bid & 3, nt = (bid >> 3) & 3, b = bid >> 5;
  const int h = hq * 8 + 2 * nt + half;
  const char* xg = (const char*)(p ? xB : xA) + (size_t)b * SQ * DM * 2;
  const char* wq = (const char*)(p ? wqB : wqA) + h * (DH * 2);
  const char* kg = (const char*)(p ? kB : kA) + (size_t)(b * NH + h) * SK * DH * 2 + w * 4096;
  const char* vg = (const char*)(p ? vB : vA) + (size_t)(b * NH + h) * SK * DH * 2 + w * 4096;
  const char* wo = (const char*)(p ? woB : woA);
  u64 tag = (hw_dispatch_id() + 1) * 0x9E3779B97F4A7C15ull ^ (u64)(uintptr_t)__builtin_amdgcn_dispatch_ptr() * 0xC2B2AE3D27D4EB4Full;
  asm volatile("" : "+s"(wo), "+s"(ws), "+s"(out), "+s"(tag), "+s"(kg), "+s"(vg));
  const unsigned vkE = (lane >> 3) * 256 + (((lane & 7) ^ (lane >> 4)) << 4), vkO = vkE ^ 64;
  const unsigned vv = (lane >> 3) * 256 + (((lane & 7) ^ (tsw(lane >> 3) << 1)) << 4);
  const unsigned lkv = lds0 + w * 4096;
  auto issue_kv2 = [&](int j, unsigned so, auto I) {
    constexpr int i = decltype(I)::value;
    const char* s = ((j & 1) ? vg : kg) + (j >> 1) * SLOT;
    dma<(i & 1) * 128 + (i >> 1) * 2048, i * 1024, 1>((j & 1) ? vv : ((i >> 1) ? vkO : vkE), s, lkv + so);
  };
  auto issue_kv = [&](int j, auto I) { issue_kv2(j, ((j + 3) % NSLOT) * SLOT, I); };
  unsigned kb = lds0 + (r32 >> 3) * 2048 + (r32 & 7) * 128 + ((hh ^ ((r32 >> 1) & 7)) << 4);
  unsigned kbx = lds0 + w * 4096 + (c >> 3) * 2048 + (c & 7) * 128 + ((g ^ (c >> 1)) << 4);

  s16x8 q[8], qx[4];
  {
    f32x4 acc[9][2];
#pragma unroll
    for (int i = 0; i < 9; ++i) { acc[i][0] = f32x4{}; acc[i][1] = f32x4{}; }
    const Gemm<DM * 2, 256, HD * 2, 128, 0, 0> G(xg, wq, lds0, ldsb, w, lane, b + nt);
    u32x4 pf[6][4];
    auto pair13 = [&](auto T, auto N, const char* swb, const char* sxb) {
      constexpr int t = decltype(T)::value, n = decltype(N)::value;
      if constexpr (n < 4) G.template issue_w<t, n>(swb);
      else if constexpr (n < 8) G.template issue_w_other<t, n - 4>(swb);
      else G.template issue_x<t, n - 8>(sxb);
    };
    auto kv_early = [&](auto N) {
      constexpr int n = decltype(N)::value, k = n >> 2, i = n & 3;
      constexpr int j = k == 0 ? 0 : k == 1 ? 0 : k == 2 ? 1 : k == 3 ? 1 : k == 4 ? 2 : k == 5 ? 2 : 3;
      constexpr bool own = k & 1;
      if constexpr (own && (j & 1) == 0) issue_kv(j, std::integral_constant<int, i>{});
      else {
        const char* sp = ((j & 1) ? vg : kg) + (j >> 1) * SLOT - (own ? 0 : SK * DH * 2);
        gld_nt<(i & 1) * 128 + (i >> 1) * 2048>(pf[own ? 4 + (j >> 1) : j][i], (j & 1) ? vv : ((i >> 1) ? vkO : vkE), sp);
      }
    };
    if (half == 0) {
      static_for<2>([&](auto T) {
        const char *swb = G.wbase(decltype(T)::value), *sxb = G.xbase(decltype(T)::value);
        static_for<13>([&](auto N) { pair13(T, N, swb, sxb); });
      });
      wait_bar<12>();
    } else wait_bar<63>();
    STAMP(6);
    const char *sxb = nullptr, *swb = nullptr;
    G.run(acc, [&](auto S, auto I) {
      constexpr int s = decltype(S)::value, i = decltype(I)::value;
      if constexpr (i == 15 && s < 7) {
        if (half == 0) wait_bar<0>(); else wait_bar<63>();
        sxb = G.xbase(s + 2); swb = G.wbase(s + 2);
      }
      constexpr int t = i >= 15 ? s + 2 : s + 1, n = i >= 15 ? i - 15 : i + 3;
      if constexpr (n < 13 && (i >= 15 ? s < 7 : s >= 1) && t < 8) {
        if (half == 0) pair13(std::integral_constant<int, t>{}, std::integral_constant<int, n>{}, swb, sxb);
      }
      if constexpr ((i & 3) == 1 && i < 16) { if (half) kv_early(std::integral_constant<int, 4 * s + (i >> 2)>{}); }
    });
    const unsigned qw = lds0 + SLOT + (c >> 3) * 2048 + (w >> 1) * 1024 + (c & 7) * 128 + (((4 * (w & 1) + g) ^ (c >> 1)) << 4);
    wait_bar<63>();
#pragma unroll
    for (int rt = 0; rt < 8; ++rt) *(LDS_AS u32x4*)(qw + rt * 4096) = c2frag(pk4(acc[rt][0]), pk4(acc[rt][1]));
    const u32x4 fx = c2frag(pk4(acc[8][0]), pk4(acc[8][1]));
    const unsigned pw = ldsb + w * 4096 + lane * 16;
    if (half) {
      asm volatile("s_waitcnt vmcnt(0)"
                   : "+v"(pf[0][0]), "+v"(pf[0][1]), "+v"(pf[0][2]), "+v"(pf[0][3]), "+v"(pf[1][0]), "+v"(pf[1][1]), "+v"(pf[1][2]), "+v"(pf[1][3]),
                     "+v"(pf[2][0]), "+v"(pf[2][1]), "+v"(pf[2][2]), "+v"(pf[2][3]), "+v"(pf[3][0]), "+v"(pf[3][1]), "+v"(pf[3][2]), "+v"(pf[3][3]),
                     "+v"(pf[4][0]), "+v"(pf[4][1]), "+v"(pf[4][2]), "+v"(pf[4][3]), "+v"(pf[5][0]), "+v"(pf[5][1]), "+v"(pf[5][2]), "+v"(pf[5][3]));
#pragma unroll
      for (int i = 0; i < 4; ++i) {
#pragma unroll
        for (int j = 0; j < 3; ++j) *(LDS_AS u32x4*)(pw + ((j + 3) % NSLOT) * SLOT + i * 1024) = pf[j][i];
        *(LDS_AS u32x4*)(pw + LDS_BYTES + 4 * SLOT + i * 1024) = pf[4][i];
      }
    }
    wait_bar<63>();
#pragma unroll
    for (int ks = 0; ks < 8; ++ks) q[ks] = lds_b128((kb ^ ((ks & 3) << 5)) + SLOT + w * 8192 + (ks >> 2) * 1024);
    asm volatile("s_waitcnt lgkmcnt(0)" : "+v"(q[0]), "+v"(q[1]), "+v"(q[2]), "+v"(q[3]), "+v"(q[4]), "+v"(q[5]), "+v"(q[6]), "+v"(q[7])::"memory");
    *(LDS_AS u32x4*)(lds0 + SLOT + w * 8192 + lane * 16) = fx;
    wait_bar<63>();
#pragma unroll
    for (int ks = 0; ks < 4; ++ks) qx[ks] = lds_b128(lds0 + SLOT + ks * 8192 + lane * 16);
    wait_bar<63>();
    if (half) {
#pragma unroll
      for (int i = 0; i < 4; ++i) {
        *(LDS_AS u32x4*)(pw + SLOT + i * 1024) = pf[3][i];
        *(LDS_AS u32x4*)(pw + LDS_BYTES + SLOT + i * 1024) = pf[5][i];
      }
    }
    static_for<4>([&](auto I) { issue_kv(4, I); });
  }

  f32x16 o[4];
  f32x4 ox[8];
#pragma unroll
  for (int i = 0; i < 4; ++i) o[i] = f32x16{};
#pragma unroll
  for (int i = 0; i < 8; ++i) ox[i] = f32x4{};
  float m, mx, l = 0.f, lx = 0.f;
  f32x16 sA, sB;
  f32x4 sx;
  u32x4 pA[2], pB[2];
  u32x2 px;
  const int vr = 4 * hh + ((lane >> 2) & 3);
  unsigned vb = lds0 + vr * 128 + (((g & 1) ^ tsw(vr)) << 5) + (lane & 3) * 8;
  const int vrx = (lane >> 2) & 7;
  unsigned vbx = lds0 + w * 4096 + (g >> 1) * 2048 + vrx * 128 + (tsw(vrx) << 5) + (lane & 3) * 8;

  s16x8 kf[4], vf[4], kx[4];
  s16x4 vx[4];
  auto ldk = [&](auto QK_, auto I, unsigned ko) {
    constexpr int i = decltype(I)::value;
    if constexpr (decltype(QK_)::value) kf[i & 3] = lds_b128(((kb + ko) ^ ((i & 3) << 5)) + (i >> 2) * 1024);
  };
  auto ldv = [&](auto PV_, auto I, unsigned vo) {
    constexpr int i = decltype(I)::value;
    if constexpr (decltype(PV_)::value) {
      const unsigned a = ((vb + vo) ^ (((i >> 1) & 1) << 6)) + (i & 1) * 4096 + (i >> 2) * 1024;
      vf[i & 3] = cat8(lds_tr(a), lds_tr(a + 2048));
    }
  };
  auto ldx = [&](auto XM_, auto I, unsigned xo) {
    constexpr int XM = decltype(XM_)::value, i = decltype(I)::value;
    if constexpr (XM == 1 && i < 4) kx[i] = lds_b128(((kbx + xo) ^ ((i & 1) << 6)) + (i >> 1) * 1024);
    if constexpr (XM == 2 || XM == 4) vx[i & 3] = lds_tr(((vbx + xo) ^ ((i & 3) << 5)) + (i >> 2) * 1024);
  };
  auto stage = [&](auto QK_, auto PV_, auto SM_, auto XM_, auto NQK_, auto NPV_, auto NXM_, auto BAR_, f32x16& sn, const f32x16& sc,
                   const u32x4 (&pc)[2], u32x4 (&pn)[2], unsigned ko, unsigned vo, unsigned xo, unsigned nko, unsigned nvo, unsigned nxo,
                   int jd, unsigned d0, unsigned d1, bool last) {
    constexpr bool QK = decltype(QK_)::value, PV = decltype(PV_)::value, SM = decltype(SM_)::value;
    constexpr int XM = decltype(XM_)::value, BAR = decltype(BAR_)::value;
    constexpr bool XPV = XM == 2 || XM == 4, XSM = XM == 2 || XM == 3;
    const float nm = -m * CEXP, nmx = -mx * CEXP;
    float ps = 0.f, psx = 0.f, e0 = 0.f, e1 = 0.f, x0 = 0.f, x1 = 0.f;
    u32x2 pxn;
    static_for<8>([&](auto I) {
      constexpr int i = decltype(I)::value;
      if constexpr (i == 6 && BAR >= 0) {
        STAMP_ACC_BEGIN;
        if constexpr (BAR == 0) wait_bar<63>();
        else { if (last) wait_bar<0>(); else wait_bar<4>(); }
        STAMP_ACC_END(8);
      }
      if constexpr (QK) sn = mfmaL(kf[i & 3], q[i], i == 0 ? f32x16{} : sn);
      using I2 = std::integral_constant<int, (i + 2) & 7>;
      if constexpr (i < 6) ldk(QK_, I2{}, ko); else ldk(NQK_, I2{}, nko);
      float f0, f1;
      if constexpr (SM) {
        f0 = fmaf(sc[2 * i], CEXP, nm); f1 = fmaf(sc[2 * i + 1], CEXP, nm);
        if constexpr (i > 0) { ps += e0 + e1; pn[(i - 1) >> 2][(i - 1) & 3] = pk2(e0, e1); asm volatile("" : "+v"(pn[(i - 1) >> 2][(i - 1) & 3]), "+v"(ps)); }
        asm volatile("" : "+v"(f0), "+v"(f1));
      }
      SB();
      if constexpr (PV) o[i >> 1] = mfmaL(vf[i & 3], __builtin_bit_cast(s16x8, pc[i & 1]), o[i >> 1]);
      if constexpr (i < 6) ldv(PV_, I2{}, vo); else ldv(NPV_, I2{}, nvo);
      if constexpr (SM) { e0 = ex2(f0); e1 = ex2(f1); asm volatile("" : "+v"(e0), "+v"(e1)); }
      SB();
      if constexpr (XM == 1 && i < 4) sx = mfma32(kx[i], qx[i], i == 0 ? f32x4{} : sx);
      if constexpr (XPV) ox[i] = mfma16(vx[i & 3], __builtin_bit_cast(s16x4, px), ox[i]);
      if constexpr (i < 6) ldx(XM_, I2{}, xo); else ldx(NXM_, I2{}, nxo);
      if constexpr (XSM && (i == 2 || i == 4)) {
        x0 = ex2(fmaf(sx[i - 2], CEXP, nmx)); x1 = ex2(fmaf(sx[i - 1], CEXP, nmx));
        asm volatile("" : "+v"(x0), "+v"(x1));
      }
      if constexpr (XSM && (i == 3 || i == 5)) {
        psx += x0 + x1; pxn[(i - 3) >> 1] = pk2(x0, x1);
        asm volatile("" : "+v"(pxn[(i - 3) >> 1]), "+v"(psx));
      }
      if constexpr (BAR >= 0 && i >= 6) { if (jd >= 0) issue_kv2(jd, d0, std::integral_constant<int, i - 6>{}); }
      if constexpr (XM == 1 && PV && i < 6) {
        if (jd >= 0 && !(i < 2 && last)) issue_kv2(jd + ((i + 2) >> 2), i < 2 ? d0 : d1, std::integral_constant<int, ((i + 2) & 3)>{});
      }
      SB();
    });
    if constexpr (SM) {
      ps += e0 + e1; pn[1][3] = pk2(e0, e1);
      if (__any(!(ps <= PSUM_LIMIT))) {
        const float mn = fmaxf(m, x32_max(max16(sc)));
        const float al = ex2((m - mn) * CEXP), nn = -mn * CEXP;
        m = mn; l *= al; ps = 0.f;
#pragma unroll
        for (int i = 0; i < 4; ++i) o[i] *= al;
#pragma unroll
        for (int i = 0; i < 8; ++i) {
          const float a0 = ex2(fmaf(sc[2 * i], CEXP, nn)), a1 = ex2(fmaf(sc[2 * i + 1], CEXP, nn));
          ps += a0 + a1;
          pn[i >> 2][i & 3] = pk2(a0, a1);
        }
      }
      l += ps;
    }
    if constexpr (XSM) {
      if (__any(!(psx <= PSUM_LIMIT))) {
        const float mn = fmaxf(mx, xg_max(fmaxf(fmaxf(sx[0], sx[1]), fmaxf(sx[2], sx[3]))));
        const float al = ex2((mx - mn) * CEXP), nn = -mn * CEXP;
        mx = mn; lx *= al;
#pragma unroll
        for (int i = 0; i < 8; ++i) ox[i] *= al;
        const float a0 = ex2(fmaf(sx[0], CEXP, nn)), a1 = ex2(fmaf(sx[1], CEXP, nn)), a2 = ex2(fmaf(sx[2], CEXP, nn)),
                    a3 = ex2(fmaf(sx[3], CEXP, nn));
        psx = (a0 + a1) + (a2 + a3);
        pxn[0] = pk2(a0, a1); pxn[1] = pk2(a2, a3);
      }
      lx += psx;
      px = pxn;
    }
  };
  using T = std::true_type; using F = std::false_type;
  using X0 = std::integral_constant<int, 0>; using X1 = std::integral_constant<int, 1>; using X2 = std::integral_constant<int, 2>;
  using X3 = std::integral_constant<int, 3>; using X4 = std::integral_constant<int, 4>; using NB_ = std::integral_constant<int, -1>;

  STAMP(2);
  m = 0.f; mx = 0.f;
  ldk(T{}, X0{}, 3 * SLOT); ldk(T{}, X1{}, 3 * SLOT); ldx(X1{}, X0{}, 3 * SLOT); ldx(X1{}, X1{}, 3 * SLOT);
  SB();
  stage(T{}, F{}, F{}, X1{}, T{}, F{}, X3{}, NB_{}, sA, sB, pB, pA, 3 * SLOT, 0, 3 * SLOT, 3 * SLOT + 8192, 0, 0, -1, 0, 0, false);
  m = x32_max(max16(sA));
  mx = xg_max(fmaxf(fmaxf(sx[0], sx[1]), fmaxf(sx[2], sx[3])));
  stage(T{}, F{}, T{}, X3{}, T{}, T{}, X1{}, X0{}, sB, sA, pB, pA, 3 * SLOT + 8192, 0, 0, 0, 4 * SLOT, 0, -1, 0, 0, false);
  unsigned cv = 4 * SLOT, ck = 0, d0 = 2 * SLOT, d1 = 3 * SLOT;
  asm volatile("" : "+v"(kb), "+v"(kbx), "+v"(vb), "+v"(vbx));
  for (int t = 0; t < 6; ++t) {
    unsigned nk = ck + 2 * SLOT, nv = cv + 2 * SLOT;
    if (nk >= NSLOT * SLOT) nk -= NSLOT * SLOT;
    if (nv >= NSLOT * SLOT) nv -= NSLOT * SLOT;
    stage(T{}, T{}, T{}, X1{}, T{}, T{}, X2{}, NB_{}, sA, sB, pA, pB, ck, cv, ck, ck + 8192, cv + 8192, cv, 2 * t + 4, d0, d1, t == 0);
    stage(T{}, T{}, T{}, X2{}, T{}, T{}, X1{}, X1{}, sB, sA, pB, pA, ck + 8192, cv + 8192, cv, nk, nv, nk, t < 5 ? 2 * t + 6 : -1, cv, 0, false);
    d0 = cv; d1 = ck; ck = nk; cv = nv;
  }
  const int ln = __builtin_amdgcn_mbcnt_hi(~0u, __builtin_amdgcn_mbcnt_lo(~0u, 0u));
  const int h0 = 8 * hq + half;
  const Gemm<DH * 2, 2 * SQ * DH * 2, DM * 2, 256, 2, 0> G(ws + (size_t)((p * NB + b) * NH + h0) * (SQ * DH * 2),
                                                                  wo + (size_t)(h0 * DH) * (DM * 2) + nt * 256, lds0, lds0, w, ln, 2 * nt);
  G.issue_w4<0>();
  {
    const char* sb = wo + (size_t)(h * DH + b * 16 + w * 4) * (DM * 2);
    dma<0, 0, 0>(ln * 16, sb, lkv); dma<1024, 1024, 0>(ln * 16, sb, lkv); dma<2048, 2048, 0>(ln * 16, sb, lkv);
    dma<3072, 3072, 0>(ln * 16, sb, lkv);
  }
  asm volatile("" : "+v"(kb), "+v"(kbx), "+v"(vb), "+v"(vbx));
  stage(T{}, T{}, T{}, X1{}, T{}, T{}, X2{}, NB_{}, sA, sB, pA, pB, 2 * SLOT, SLOT, 2 * SLOT, 2 * SLOT + 8192, SLOT + 8192, SLOT, -1, 0, 0, false);
  stage(T{}, T{}, T{}, X2{}, F{}, T{}, X4{}, X1{}, sB, sA, pB, pA, 2 * SLOT + 8192, SLOT + 8192, SLOT, 0, 3 * SLOT, 3 * SLOT, -1, 0, 0, true);
  stage(F{}, T{}, T{}, X4{}, F{}, T{}, X0{}, NB_{}, sA, sB, pA, pB, 0, 3 * SLOT, 3 * SLOT, 0, 3 * SLOT + 8192, 0, -1, 0, 0, false);
  stage(F{}, T{}, F{}, X0{}, F{}, F{}, X0{}, NB_{}, sB, sA, pB, pA, 0, 3 * SLOT + 8192, 0, 0, 0, 0, -1, 0, 0, false);

  STAMP(4);
  const int g2 = ln >> 4, c2 = ln & 15;
  u64* flagO = (u64*)(ws + WS_FLAG);
  u64* flagS = flagO + 256 * 8;
  const auto rsO = __builtin_amdgcn_make_buffer_rsrc(ws, 0, WS_SLAB, 0x00020000);
  const auto rsS = __builtin_amdgcn_make_buffer_rsrc(ws + WS_SLAB, 0, WS_FLAG - WS_SLAB, 0x00020000);
  const unsigned ob = ((p * NB + b) * NH + h) * (SQ * DH * 2);
  lx = xg_sum(lx);
  wait_bar<0>();
  STAMPR(9);
  {
    const float rl = 1.f / x32_sum(l);
    const int r = ln & 31;
    const unsigned sw = lds0 + (4 * w + (r >> 3)) * 1024 + (r & 7) * 128;
#pragma unroll
    for (int db = 0; db < 4; ++db)
#pragma unroll
      for (int k = 0; k < 4; k += 2) {
        const unsigned a0 = pk2(o[db][4 * k] * rl, o[db][4 * k + 1] * rl), a1 = pk2(o[db][4 * k + 2] * rl, o[db][4 * k + 3] * rl);
        const unsigned b0 = pk2(o[db][4 * k + 4] * rl, o[db][4 * k + 5] * rl), b1 = pk2(o[db][4 * k + 6] * rl, o[db][4 * k + 7] * rl);
        auto r0 = __builtin_amdgcn_permlane32_swap(a0, b0, false, false);
        auto r1 = __builtin_amdgcn_permlane32_swap(a1, b1, false, false);
        *(LDS_AS u32x4*)(sw + XB[db >> 1] + (((4 * (db & 1) + k + (ln >> 5)) ^ ((r >> 1) & 7)) << 4)) = (u32x4){r0[0], r1[0], r0[1], r1[1]};
      }
#pragma unroll
    for (int dt = 0; dt < 8; ++dt)
      if ((dt >> 1) != w) *(LDS_AS f32x4*)(lds0 + SCR + (((dt >> 1) * 3 + ((w - (dt >> 1) - 1) & 3)) * 2 + (dt & 1)) * 1024 + ln * 16) = ox[dt];
    if (g2 == 0) { *(LDS_AS f32x2*)(lds0 + SCR + 24576 + w * 128 + c2 * 8) = (f32x2){mx, lx}; }
  }
  STAMPR(10);
  wait_bar<63>();
  STAMPR(11);
  {
    f32x2 ml[4];
#pragma unroll
    for (int i = 0; i < 4; ++i) ml[i] = *(const LDS_AS f32x2*)(lds0 + SCR + 24576 + i * 128 + c2 * 8);
    const float M = fmaxf(fmaxf(ml[0][0], ml[1][0]), fmaxf(ml[2][0], ml[3][0]));
    float L = 0.f;
#pragma unroll
    for (int i = 0; i < 4; ++i) L += ex2((ml[i][0] - M) * CEXP) * ml[i][1];
    const float rl = 1.f / L;
    f32x4 a0 = w == 0 ? ox[0] : w == 1 ? ox[2] : w == 2 ? ox[4] : ox[6], a1 = w == 0 ? ox[1] : w == 1 ? ox[3] : w == 2 ? ox[5] : ox[7];
    const float so = ex2((mx - M) * CEXP) * rl;
    a0 *= so; a1 *= so;
#pragma unroll
    for (int i = 0; i < 3; ++i) {
      const f32x2 mi = *(const LDS_AS f32x2*)(lds0 + SCR + 24576 + ((w + 1 + i) & 3) * 128 + c2 * 8);
      const float si = ex2((mi[0] - M) * CEXP) * rl;
      const unsigned pb = lds0 + SCR + (w * 3 + i) * 2048 + ln * 16;
      a0 += *(const LDS_AS f32x4*)(pb) * si;
      a1 += *(const LDS_AS f32x4*)(pb + 1024) * si;
    }
    const u32x4 v = c2frag(pk4(a0), pk4(a1));
    *(LDS_AS u32x4*)(lds0 + ((w >> 1) ? XB[1] : XB[0]) + (16 + (c2 >> 3)) * 1024 + (c2 & 7) * 128 + (((4 * (w & 1) + g2) ^ (c2 >> 1)) << 4)) = v;
    __builtin_amdgcn_raw_buffer_store_b128(v, rsO, ob + (128 + c2) * 256 + w * 64 + g2 * 16, 0, 16);
  }
  STAMP(5);
  f32x4 acc[9][2];
#pragma unroll
  for (int i = 0; i < 9; ++i) { acc[i][0] = f32x4{}; acc[i][1] = f32x4{}; }
  const unsigned foff = ((bid & ~24) | (((nt + ln) & 3) << 3)) * 64;
  u32x2 fl;
  auto fl_load = [&]() { gld_flag(fl, foff, (const char*)flagO); };
  auto fl_bit = [&](int k) {
    asm volatile("" : "+v"(fl));
    return (bool)((__ballot((((u64)fl[1] << 32) | fl[0]) == tag) >> k) & 1);
  };
  auto fl_spin = [&](int k) {
    const gu64* f = (const gu64*)((const char*)flagO + __builtin_amdgcn_readlane(foff, k));
    for (int spins = 0; spins < (1 << 17); ++spins) {
      if (__hip_atomic_load(f, __ATOMIC_RELAXED, __HIP_MEMORY_SCOPE_AGENT) == tag) break;
      __builtin_amdgcn_s_sleep(2);
    }
  };
  wait_bar<63>();
  const char *sxb = nullptr, *swb = G.wbase(1);
  u32x4 ov;
  G.run(acc, [&](auto S, auto I) {
    constexpr int s = decltype(S)::value, i = decltype(I)::value;
    if constexpr (i == 0 && (s & 1) == 0 && s < 6) fl_load();
    if constexpr (s == 0) {
      if constexpr (i < 4) G.template issue_w<1, i>(swb);
      if constexpr (i >= 5 && i < 13) {
        constexpr int k = i - 5;
        __builtin_amdgcn_raw_buffer_store_b128(
            ov, rsO, ob + (32 * w + 8 * (k & 3) + (ln >> 3)) * 256 + (k >> 2) * 128 + (((ln & 7) ^ ((4 * (k & 3) + g2) & 7)) << 4), 0, 16);
      }
      if constexpr (i >= 4 && i < 12) ov = *(const LDS_AS u32x4*)(lds0 + XB[(i - 4) >> 2] + (4 * w + ((i - 4) & 3)) * 1024 + ln * 16);
    }
    if constexpr (i == 15 && s < 7) {
      wait_bar<0>();
      if constexpr (s == 0) {
        if (threadIdx.x == 0)
          __hip_atomic_store((gu64*)(flagO + bid * 8), tag, __ATOMIC_RELAXED, __HIP_MEMORY_SCOPE_AGENT);
        STAMPR(12);
      }
      if constexpr ((s & 1) == 0 && s < 6) { if (!fl_bit((s + 2) >> 1)) fl_spin((s + 2) >> 1); }
      if constexpr (s == 0) STAMPR(13);
      if constexpr (s == 1) STAMP(7);
      if constexpr (s == 3) STAMPR(14);
      sxb = G.xbase(s + 2); swb = G.wbase(s + 2);
    }
    constexpr int t = i >= 15 ? s + 2 : s + 1, n = i >= 15 ? i - 15 : i + 3;
    if constexpr (n < 9 && (i >= 15 ? s < 7 : s >= 1) && t < 8) G.template issue<t, n>(swb, sxb);
  });
  STAMP(3);
  wait_bar<63>();
  {
    const unsigned xw = lds0 + w * 2048 + ln * 16, xr = ldsb + (half ^ 1) * LDS_BYTES + w * 2048 + ln * 16;
    if (half == 0) {
#pragma unroll
      for (int rt = 5; rt < 9; ++rt) { *(LDS_AS f32x4*)(xw + rt * 8192) = acc[rt][0]; *(LDS_AS f32x4*)(xw + rt * 8192 + 1024) = acc[rt][1]; }
    } else {
#pragma unroll
      for (int rt = 0; rt < 5; ++rt) { *(LDS_AS f32x4*)(xw + rt * 8192) = acc[rt][0]; *(LDS_AS f32x4*)(xw + rt * 8192 + 1024) = acc[rt][1]; }
    }
    wait_bar<63>();
    const int tile = (p * NB + b) * 4 + nt;
    const int own0 = hq == 0 ? 0 : 2 * hq + 1, own1 = 2 * hq + 3;
    const unsigned sb = (unsigned)tile * (4 * 73728) + w * 2048 + ln * 16;
#pragma unroll
    for (int rt = 0; rt < 9; ++rt)
      if ((rt >= 5) == (half != 0)) {
        const f32x4 o0 = *(const LDS_AS f32x4*)(xr + rt * 8192), o1 = *(const LDS_AS f32x4*)(xr + rt * 8192 + 1024);
        acc[rt][0] = rt < 5 ? acc[rt][0] + o0 : o0 + acc[rt][0];
        acc[rt][1] = rt < 5 ? acc[rt][1] + o1 : o1 + acc[rt][1];
        if (rt < own0 || rt >= own1) {
          __builtin_amdgcn_raw_buffer_store_b128(__builtin_bit_cast(u32x4, acc[rt][0]), rsS, sb + hq * 73728 + rt * 8192, 0, 16);
          __builtin_amdgcn_raw_buffer_store_b128(__builtin_bit_cast(u32x4, acc[rt][1]), rsS, sb + hq * 73728 + rt * 8192 + 1024, 0, 16);
        }
      }
    publish(flagS + (tile * 4 + hq) * 8, tag);
    await(flagS + tile * 32, 8, 4, tag, w8, ln, false);
    u16* orow = out + (size_t)(b * SQ + c2) * (2 * DM) + p * DM + nt * 128 + 32 * w + 8 * g2;
#pragma unroll
    for (int rt = 0; rt < 9; ++rt)
      if ((rt >= 5) == (half != 0) && rt >= own0 && rt < own1) {
        f32x4 t0[4], t1[4];
#pragma unroll
        for (int k = 0; k < 4; ++k) {
          const unsigned a = sb + (k == hq ? 0x7f000000u : k * 73728 + rt * 8192);
          t0[k] = __builtin_bit_cast(f32x4, __builtin_amdgcn_raw_buffer_load_b128(rsS, a, 0, 16));
          t1[k] = __builtin_bit_cast(f32x4, __builtin_amdgcn_raw_buffer_load_b128(rsS, a + 1024, 0, 16));
        }
        f32x4 s0 = f32x4{}, s1 = f32x4{};
#pragma unroll
        for (int k = 0; k < 4; ++k) {
          s0 += k == hq ? acc[rt][0] : t0[k];
          s1 += k == hq ? acc[rt][1] : t1[k];
        }
        *(u32x4*)(orow + (size_t)(16 * rt) * (2 * DM)) = c2frag(pk4(s0), pk4(s1));
      }
  }
  STAMP(1); STAMP_FLUSH(0);
}

extern "C" void kernel_launch(void* const* d_in, const int* in_sizes, int n_in, void* d_out, int out_size, void* d_ws, size_t ws_size,
                              hipStream_t stream) {
  (void)in_sizes; (void)n_in; (void)out_size; (void)ws_size;
  const u16* const* in = (const u16* const*)d_in;
  (void)hipFuncSetAttribute((const void*)mha_fused, hipFuncAttributeMaxDynamicSharedMemorySize, 2 * LDS_BYTES);
  mha_fused<<<256, 512, 2 * LDS_BYTES, stream>>>(in[0], in[1], in[2], in[3], in[4], in[5], in[6], in[7], in[8], in[9], (char*)d_ws,
                                             (u16*)d_out);
}
```

```cpp
#include <hip/hip_runtime.h>
#include <hip/hip_bf16.h>
#include <stdint.h>
#include <type_traits>

typedef unsigned short u16;
typedef __attribute__((ext_vector_type(8))) short s16x8;
typedef __attribute__((ext_vector_type(4))) short s16x4;
typedef __attribute__((ext_vector_type(16))) float f32x16;
typedef __attribute__((ext_vector_type(4))) float f32x4;
typedef __attribute__((ext_vector_type(2))) float f32x2;
typedef __attribute__((ext_vector_type(4))) unsigned u32x4;
typedef __attribute__((ext_vector_type(2))) unsigned u32x2;
typedef __attribute__((ext_vector_type(2))) __bf16 bf16x2_t;
#define LDS_AS __attribute__((address_space(3)))
#define DI __device__ __forceinline__
#define SB() __builtin_amdgcn_sched_barrier(0)

constexpr int NB = 8, SQ = 144, DM = 512, NH = 32, SK = 512, DH = 128, HD = NH * DH;
constexpr int SLOT = 16384, NSLOT = 5, LDS_BYTES = SLOT * NSLOT;
constexpr unsigned WS_SLAB = 2u * NB * NH * SQ * DH * 2, WS_FLAG = WS_SLAB + 64u * 8 * 73728;
constexpr float CEXP = 0.08838834764831845f * 1.4426950408889634f;
constexpr float PSUM_LIMIT = 4096.f;

#define STAMP_DECL
#define STAMP_ARG
#define STAMP_PASS
#define STAMP(I) do {} while (0)
#define STAMPR(I) do {} while (0)
#define STAMP_ACC_BEGIN
#define STAMP_ACC_END(I) do {} while (0)
#define STAMP_FLUSH(K) do {} while (0)

#define W_NT false
#define WO_NT false
#define O_NT false
#define ROT1 (b + h)
#define ROT2 (b + 2 * nt)
#define GRID1 512
#define LD_SLOT 0
#define PRIO 1
#define PRE_ON 0
#define ACQ_ON true
#define QX_POL 0
#define QW_POL 0
#define OW_POL 0
#define OX_POL 2
#define ST_AUX 16
#define X_NT false
template <int N, class F> DI void static_for(F&& f) {
  if constexpr (N > 0) { static_for<N - 1>(f); f(std::integral_constant<int, N - 1>{}); }
}

DI f32x16 mfmaL(s16x8 a, s16x8 b, f32x16 c) { return __builtin_amdgcn_mfma_f32_32x32x16_bf16(a, b, c, 0, 0, 0); }
DI f32x4 mfma32(s16x8 a, s16x8 b, f32x4 c) { return __builtin_amdgcn_mfma_f32_16x16x32_bf16(a, b, c, 0, 0, 0); }
DI f32x4 mfma16(s16x4 a, s16x4 b, f32x4 c) { return __builtin_amdgcn_mfma_f32_16x16x16bf16_1k(a, b, c, 0, 0, 0); }
DI s16x8 lds_b128(unsigned a) { return *(const LDS_AS s16x8*)(a); }
DI s16x4 lds_tr(unsigned a) { return __builtin_amdgcn_ds_read_tr16_b64_v4i16((LDS_AS s16x4*)(a)); }
DI s16x8 cat8(s16x4 lo, s16x4 hi) { return __builtin_shufflevector(lo, hi, 0, 1, 2, 3, 4, 5, 6, 7); }
DI unsigned pk2(float a, float b) { f32x2 v = {a, b}; return __builtin_bit_cast(unsigned, __builtin_convertvector(v, bf16x2_t)); }
DI u32x2 pk4(f32x4 v) { u32x2 r = {pk2(v[0], v[1]), pk2(v[2], v[3])}; return r; }
DI float ex2(float x) { return __builtin_amdgcn_exp2f(x); }

DI u32x4 c2frag(u32x2 t0, u32x2 t1) {
  u32x4 r;
#pragma unroll
  for (int i = 0; i < 2; ++i) {
    auto a = __builtin_amdgcn_permlane32_swap(t0[i], t1[i], false, false);
    auto b = __builtin_amdgcn_permlane16_swap(a[0], a[1], false, false);
    r[i] = b[0]; r[2 + i] = b[1];
  }
  return r;
}
DI float x32_max(float x) {
  auto b = __builtin_amdgcn_permlane32_swap(__float_as_uint(x), __float_as_uint(x), false, false);
  return fmaxf(__uint_as_float(b[0]), __uint_as_float(b[1]));
}
DI float x32_sum(float x) {
  auto b = __builtin_amdgcn_permlane32_swap(__float_as_uint(x), __float_as_uint(x), false, false);
  return __uint_as_float(b[0]) + __uint_as_float(b[1]);
}
DI float xg_max(float x) {
  auto a = __builtin_amdgcn_permlane16_swap(__float_as_uint(x), __float_as_uint(x), false, false);
  return x32_max(fmaxf(__uint_as_float(a[0]), __uint_as_float(a[1])));
}
DI float xg_sum(float x) {
  auto a = __builtin_amdgcn_permlane16_swap(__float_as_uint(x), __float_as_uint(x), false, false);
  return x32_sum(__uint_as_float(a[0]) + __uint_as_float(a[1]));
}
DI float max16(const f32x16& s) {
  float a = fmaxf(fmaxf(s[0], s[1]), s[2]), b = fmaxf(fmaxf(s[3], s[4]), s[5]);
  a = fmaxf(fmaxf(a, s[6]), s[7]); b = fmaxf(fmaxf(b, s[8]), s[9]);
  a = fmaxf(fmaxf(a, s[10]), s[11]); b = fmaxf(fmaxf(b, s[12]), s[13]);
  return fmaxf(fmaxf(a, s[14]), fmaxf(b, s[15]));
}

template <int GOFF, int LOFF, int POL>
DI void dma(unsigned voff, const char* sb, unsigned lb) {
  if constexpr (POL == 1)
    asm volatile("s_add_u32 m0, %2, %3\n\ts_nop 0\n\tglobal_load_lds_dwordx4 %0, %1 offset:%4 nt"
                 ::"v"(voff), "s"(sb), "s"(lb), "i"(LOFF - GOFF), "i"(GOFF) : "memory", "scc", "m0");
  else if constexpr (POL == 2)
    asm volatile("s_add_u32 m0, %2, %3\n\ts_nop 0\n\tglobal_load_lds_dwordx4 %0, %1 offset:%4 sc1"
                 ::"v"(voff), "s"(sb), "s"(lb), "i"(LOFF - GOFF), "i"(GOFF) : "memory", "scc", "m0");
  else
    asm volatile("s_add_u32 m0, %2, %3\n\ts_nop 0\n\tglobal_load_lds_dwordx4 %0, %1 offset:%4"
                 ::"v"(voff), "s"(sb), "s"(lb), "i"(LOFF - GOFF), "i"(GOFF) : "memory", "scc", "m0");
}
typedef unsigned long long u64;
typedef __attribute__((address_space(1))) u64 gu64;
extern "C" __device__ u64 hw_dispatch_id() __asm("llvm.amdgcn.dispatch.id");
DI void publish(u64* flag, u64 tag) {
  asm volatile("s_waitcnt vmcnt(0) lgkmcnt(0)\n\ts_barrier" ::: "memory");
  if (threadIdx.x == 0) __hip_atomic_store((gu64*)flag, tag, __ATOMIC_RELAXED, __HIP_MEMORY_SCOPE_AGENT);
}
DI void await(const u64* flags, int stride, int n, u64 tag, int w, int lane, bool acquire) {
  if (w == 0) {
    const gu64* f = (const gu64*)flags + (lane < n ? lane : 0) * stride;
    for (int spins = 0; spins < (1 << 20); ++spins) {
      if (__all(__hip_atomic_load(f, __ATOMIC_RELAXED, __HIP_MEMORY_SCOPE_AGENT) == tag)) break;
      __builtin_amdgcn_s_sleep(2);
    }
    if (acquire) __builtin_amdgcn_fence(__ATOMIC_ACQUIRE, "agent");
  }
  asm volatile("s_waitcnt vmcnt(0) lgkmcnt(0)\n\ts_barrier" ::: "memory");
}
template <int OFF> DI void gld_sc1(u32x4& d, unsigned voff, const char* sb) {
  asm volatile("global_load_dwordx4 %0, %1, %2 offset:%3 sc1" : "=v"(d) : "v"(voff), "s"(sb), "i"(OFF));
}
DI void gld_flag(u32x2& d, unsigned voff, const char* sb) {
  asm volatile("global_load_dwordx2 %0, %1, %2 sc1" : "=v"(d) : "v"(voff), "s"(sb));
}
template <int N> DI void wait_bar() { asm volatile("s_waitcnt vmcnt(%0) lgkmcnt(0)\n\ts_barrier" ::"i"(N) : "memory"); }
template <int N> DI void wait_vm() { asm volatile("s_waitcnt vmcnt(%0)" ::"i"(N) : "memory"); }

DI int tsw(int r) { return (r & 2) | ((r >> 2) & 1); }
constexpr int XB[2] = {47104, 0}, WB[2] = {4 * SLOT, 18432}, SCR = 18432;

template <int AST, int ABLK, int WST, int WBLK, int XPOL, int WPOL>
struct Gemm {
  unsigned va[5], vw[2], lw, lx, lx4, bx0, bx1, bw0, bw1;
  const char *ag, *wsrc;
  int rot;
  bool x5;
  DI Gemm(const char* ag_, const char* wsrc_, unsigned lds0, int w, int lane, int rot_) : ag(ag_), wsrc(wsrc_), rot(rot_) {
    const int g = lane >> 4, c = lane & 15, pos = lane >> 3, row = (lane >> 2) & 7;
    const unsigned va0 = ((lane >> 3) * AST + (((lane & 7) ^ (lane >> 4)) << 4)) ^ ((w & 1) << 6);
#pragma unroll
    for (int i = 0; i < 4; ++i) va[i] = va0 + (8 * w + 32 * i) * AST;
    va[4] = va0 + (128 + 8 * (w & 1)) * AST;
    vw[0] = (16 * w + (pos & 3) + 8 * (pos >> 2)) * WST + (((lane & 7) ^ (tsw(pos) << 1)) << 4); vw[1] = vw[0] + 4 * WST;
    x5 = w < 2;
    lw = lds0 + w * 4096; lx = lds0 + w * 1024; lx4 = lds0 + 16384 + (w & 1) * 1024;
    bx0 = lds0 + (c >> 3) * 1024 + (c & 7) * 128 + ((g ^ (c >> 1)) << 4); bx1 = bx0 ^ 64;
    bw0 = lds0 + (g >> 1) * 4096 + (w >> 1) * 1024 + row * 128 + (((2 * (w & 1)) ^ tsw(row)) << 5) + (lane & 3) * 8; bw1 = bw0 ^ 32;
  }
  DI const char* wbase(int t) const { const int tr = (t + rot) & 7; return wsrc + (size_t)((tr >> 1) * WBLK + (tr & 1) * 64) * WST; }
  DI const char* xbase(int t) const { const int tr = (t + rot) & 7; return ag + (tr >> 1) * ABLK + (tr & 1) * 128; }
  template <int T, int I> DI void issue_w(const char* sb) const { dma<(I & 1) * 128, WB[T & 1] + I * 1024, WPOL>(vw[I >> 1], sb, lw); }
  template <int T, int I> DI void issue_x(const char* sb) const {
    if constexpr (I < 4) dma<0, XB[T & 1] + I * 4096, XPOL>(va[I], sb, lx);
    else { if (x5) dma<0, XB[T & 1], XPOL>(va[4], sb, lx4); }
  }
  template <int T> DI void issue_w4() const {
    const char* sb = wbase(T);
    issue_w<T, 0>(sb); issue_w<T, 1>(sb); issue_w<T, 2>(sb); issue_w<T, 3>(sb);
  }
  template <int T> DI void issue_x5() const {
    const char* sb = xbase(T);
    issue_x<T, 0>(sb); issue_x<T, 1>(sb); issue_x<T, 2>(sb); issue_x<T, 3>(sb); issue_x<T, 4>(sb);
  }
  template <int T, int N> DI void issue(const char* swb, const char* sxb) const {
    if constexpr (N < 4) issue_w<T, N>(swb); else issue_x<T, N - 4>(sxb);
  }
  template <class Hook> DI void run(f32x4 (&acc)[9][2], Hook&& hook) const {
    s16x8 xf[4], wf[2][2][2];
    auto ldx = [&](auto N) {
      constexpr int n = decltype(N)::value, s = n / 18, i = n % 18;
      if constexpr (n < 144) xf[n & 3] = lds_b128((i >= 9 ? bx1 : bx0) + XB[s & 1] + (i % 9) * 2048);
    };
    auto ldw = [&](auto S, auto KS) {
      constexpr int s = decltype(S)::value, ks = decltype(KS)::value, wo = WB[s & 1] + ks * 8192;
      wf[s & 1][ks][0] = cat8(lds_tr(bw0 + wo), lds_tr(bw0 + wo + 2048));
      wf[s & 1][ks][1] = cat8(lds_tr(bw1 + wo), lds_tr(bw1 + wo + 2048));
    };
    using Z = std::integral_constant<int, 0>; using O1 = std::integral_constant<int, 1>;
    ldw(Z{}, Z{});
    static_for<3>(ldx);
    SB();
    static_for<144>([&](auto N) {
      constexpr int n = decltype(N)::value, s = n / 18, i = n % 18;
      hook(std::integral_constant<int, s>{}, std::integral_constant<int, i>{});
      if constexpr (i == 15 && s < 7) ldw(std::integral_constant<int, s + 1>{}, Z{});
      if constexpr (i == 3) ldw(std::integral_constant<int, s>{}, O1{});
      ldx(std::integral_constant<int, n + 3>{});
      acc[i % 9][0] = mfma32(wf[s & 1][i / 9][0], xf[n & 3], acc[i % 9][0]);
      acc[i % 9][1] = mfma32(wf[s & 1][i / 9][1], xf[n & 3], acc[i % 9][1]);
      asm volatile("" : "+v"(acc[i % 9][0]), "+v"(acc[i % 9][1]));
      SB();
    });
  }
};

__global__ __launch_bounds__(256, 2) void mha_fused(const u16* __restrict__ xA, const u16* __restrict__ kA, const u16* __restrict__ vA,
                                                    const u16* __restrict__ wqA, const u16* __restrict__ woA, const u16* __restrict__ xB,
                                                    const u16* __restrict__ kB, const u16* __restrict__ vB, const u16* __restrict__ wqB,
                                                    const u16* __restrict__ woB, char* ws, u16* out) {
  extern __shared__ __attribute__((aligned(128))) char smem[];
  const unsigned lds0 = (unsigned)(uintptr_t)(LDS_AS char*)smem;
  const int lane = threadIdx.x & 63;
  const int w = __builtin_amdgcn_readfirstlane(threadIdx.x >> 6);
  const int g = lane >> 4, c = lane & 15, r32 = lane & 31, hh = lane >> 5;
  STAMP_DECL; STAMP(0);
  const int bid = blockIdx.x, p = (bid >> 2) & 1, sl = (bid >> 3) & 31;
  const int h = (bid & 3) * 8 + 2 * (sl & 3) + (bid >> 8), b = sl >> 2;
  const char* xg = (const char*)(p ? xB : xA) + (size_t)b * SQ * DM * 2;
  const char* wq = (const char*)(p ? wqB : wqA) + h * (DH * 2);
  const char* kg = (const char*)(p ? kB : kA) + (size_t)(b * NH + h) * SK * DH * 2 + w * 4096;
  const char* vg = (const char*)(p ? vB : vA) + (size_t)(b * NH + h) * SK * DH * 2 + w * 4096;
  const char* wo = (const char*)(p ? woB : woA);
  u64 tag = (hw_dispatch_id() + 1) * 0x9E3779B97F4A7C15ull ^ (u64)(uintptr_t)__builtin_amdgcn_dispatch_ptr() * 0xC2B2AE3D27D4EB4Full;
  asm volatile("" : "+s"(wo), "+s"(ws), "+s"(out), "+s"(tag), "+s"(kg), "+s"(vg));
  const unsigned vkE = (lane >> 3) * 256 + (((lane & 7) ^ (lane >> 4)) << 4), vkO = vkE ^ 64;
  const unsigned vv = (lane >> 3) * 256 + (((lane & 7) ^ (tsw(lane >> 3) << 1)) << 4);
  const unsigned lkv = lds0 + w * 4096;
  auto issue_kv2 = [&](int j, unsigned so, auto I) {
    constexpr int i = decltype(I)::value;
    const char* s = ((j & 1) ? vg : kg) + (j >> 1) * SLOT;
    dma<(i & 1) * 128 + (i >> 1) * 2048, i * 1024, 1>((j & 1) ? vv : ((i >> 1) ? vkO : vkE), s, lkv + so);
  };
  auto issue_kv = [&](int j, auto I) { issue_kv2(j, ((j + 3) % NSLOT) * SLOT, I); };
  unsigned kb = lds0 + (r32 >> 3) * 2048 + (r32 & 7) * 128 + ((hh ^ ((r32 >> 1) & 7)) << 4);
  unsigned kbx = lds0 + w * 4096 + (c >> 3) * 2048 + (c & 7) * 128 + ((g ^ (c >> 1)) << 4);

  __builtin_amdgcn_s_setprio(1);
  s16x8 q[8], qx[4];
  {
    f32x4 acc[9][2];
#pragma unroll
    for (int i = 0; i < 9; ++i) { acc[i][0] = f32x4{}; acc[i][1] = f32x4{}; }
    const Gemm<DM * 2, 256, HD * 2, 128, QX_POL, QW_POL> G(xg, wq, lds0, w, lane, b + h);
    G.issue_w4<0>(); G.issue_x5<0>(); G.issue_w4<1>(); G.issue_x5<1>();
    wait_bar<8>();
    STAMP(6);
    const char *sxb = nullptr, *swb = nullptr;
    G.run(acc, [&](auto S, auto I) {
      constexpr int s = decltype(S)::value, i = decltype(I)::value;
      if constexpr (i == 15 && s < 7) { wait_bar<0>(); sxb = G.xbase(s + 2); swb = G.wbase(s + 2); }
      constexpr int t = i >= 15 ? s + 2 : s + 1, n = i >= 15 ? i - 15 : i + 3;
      if constexpr (n < 9 && (i >= 15 ? s < 7 : s >= 1)) {
        if constexpr (t < 8) G.template issue<t, n>(swb, sxb);
        else if constexpr (n < 8) issue_kv(n >> 2, std::integral_constant<int, (n & 3)>{});
      }
    });
    const unsigned qw = lds0 + SLOT + (c >> 3) * 2048 + (w >> 1) * 1024 + (c & 7) * 128 + (((4 * (w & 1) + g) ^ (c >> 1)) << 4);
    wait_bar<63>();
    static_for<4>([&](auto I) { issue_kv(2, I); });
#pragma unroll
    for (int rt = 0; rt < 8; ++rt) *(LDS_AS u32x4*)(qw + rt * 4096) = c2frag(pk4(acc[rt][0]), pk4(acc[rt][1]));
    const u32x4 fx = c2frag(pk4(acc[8][0]), pk4(acc[8][1]));
    wait_bar<63>();
#pragma unroll
    for (int ks = 0; ks < 8; ++ks) q[ks] = lds_b128((kb ^ ((ks & 3) << 5)) + SLOT + w * 8192 + (ks >> 2) * 1024);
    asm volatile("s_waitcnt lgkmcnt(0)" : "+v"(q[0]), "+v"(q[1]), "+v"(q[2]), "+v"(q[3]), "+v"(q[4]), "+v"(q[5]), "+v"(q[6]), "+v"(q[7])::"memory");
    *(LDS_AS u32x4*)(lds0 + SLOT + w * 8192 + lane * 16) = fx;
    wait_bar<8>();
#pragma unroll
    for (int ks = 0; ks < 4; ++ks) qx[ks] = lds_b128(lds0 + SLOT + ks * 8192 + lane * 16);
  }

  f32x16 o[4];
  f32x4 ox[8];
#pragma unroll
  for (int i = 0; i < 4; ++i) o[i] = f32x16{};
#pragma unroll
  for (int i = 0; i < 8; ++i) ox[i] = f32x4{};
  float m, mx, l = 0.f, lx = 0.f;
  f32x16 sA, sB;
  f32x4 sx;
  u32x4 pA[2], pB[2];
  u32x2 px;
  const int vr = 4 * hh + ((lane >> 2) & 3);
  unsigned vb = lds0 + vr * 128 + (((g & 1) ^ tsw(vr)) << 5) + (lane & 3) * 8;
  const int vrx = (lane >> 2) & 7;
  unsigned vbx = lds0 + w * 4096 + (g >> 1) * 2048 + vrx * 128 + (tsw(vrx) << 5) + (lane & 3) * 8;

  s16x8 kf[4], vf[4], kx[4];
  s16x4 vx[4];
  auto ldk = [&](auto QK_, auto I, unsigned ko) {
    constexpr int i = decltype(I)::value;
    if constexpr (decltype(QK_)::value) kf[i & 3] = lds_b128(((kb + ko) ^ ((i & 3) << 5)) + (i >> 2) * 1024);
  };
  auto ldv = [&](auto PV_, auto I, unsigned vo) {
    constexpr int i = decltype(I)::value;
    if constexpr (decltype(PV_)::value) {
      const unsigned a = ((vb + vo) ^ (((i >> 1) & 1) << 6)) + (i & 1) * 4096 + (i >> 2) * 1024;
      vf[i & 3] = cat8(lds_tr(a), lds_tr(a + 2048));
    }
  };
  auto ldx = [&](auto XM_, auto I, unsigned xo) {
    constexpr int XM = decltype(XM_)::value, i = decltype(I)::value;
    if constexpr (XM == 1 && i < 4) kx[i] = lds_b128(((kbx + xo) ^ ((i & 1) << 6)) + (i >> 1) * 1024);
    if constexpr (XM == 2 || XM == 4) vx[i & 3] = lds_tr(((vbx + xo) ^ ((i & 3) << 5)) + (i >> 2) * 1024);
  };
  auto stage = [&](auto QK_, auto PV_, auto SM_, auto XM_, auto NQK_, auto NPV_, auto NXM_, auto BAR_, f32x16& sn, const f32x16& sc,
                   const u32x4 (&pc)[2], u32x4 (&pn)[2], unsigned ko, unsigned vo, unsigned xo, unsigned nko, unsigned nvo, unsigned nxo,
                   int jd, unsigned d0, unsigned d1, bool last) {
    constexpr bool QK = decltype(QK_)::value, PV = decltype(PV_)::value, SM = decltype(SM_)::value;
    constexpr int XM = decltype(XM_)::value, BAR = decltype(BAR_)::value;
    constexpr bool XPV = XM == 2 || XM == 4, XSM = XM == 2 || XM == 3;
    const float nm = -m * CEXP, nmx = -mx * CEXP;
    float ps = 0.f, psx = 0.f, e0 = 0.f, e1 = 0.f, x0 = 0.f, x1 = 0.f;
    u32x2 pxn;
    static_for<8>([&](auto I) {
      constexpr int i = decltype(I)::value;
      if constexpr (i == 6 && BAR >= 0) {
        STAMP_ACC_BEGIN;
        if constexpr (BAR == 0) { wait_bar<0>(); static_for<4>([&](auto J) { issue_kv(3, J); }); }
        else { if (last) wait_bar<0>(); else wait_bar<4>(); }
        STAMP_ACC_END(8);
      }
      if constexpr (QK) sn = mfmaL(kf[i & 3], q[i], i == 0 ? f32x16{} : sn);
      using I2 = std::integral_constant<int, (i + 2) & 7>;
      if constexpr (i < 6) ldk(QK_, I2{}, ko); else ldk(NQK_, I2{}, nko);
      float f0, f1;
      if constexpr (SM) {
        f0 = fmaf(sc[2 * i], CEXP, nm); f1 = fmaf(sc[2 * i + 1], CEXP, nm);
        if constexpr (i > 0) { ps += e0 + e1; pn[(i - 1) >> 2][(i - 1) & 3] = pk2(e0, e1); asm volatile("" : "+v"(pn[(i - 1) >> 2][(i - 1) & 3]), "+v"(ps)); }
        asm volatile("" : "+v"(f0), "+v"(f1));
      }
      SB();
      if constexpr (PV) o[i >> 1] = mfmaL(vf[i & 3], __builtin_bit_cast(s16x8, pc[i & 1]), o[i >> 1]);
      if constexpr (i < 6) ldv(PV_, I2{}, vo); else ldv(NPV_, I2{}, nvo);
      if constexpr (SM) { e0 = ex2(f0); e1 = ex2(f1); asm volatile("" : "+v"(e0), "+v"(e1)); }
      SB();
      if constexpr (XM == 1 && i < 4) sx = mfma32(kx[i], qx[i], i == 0 ? f32x4{} : sx);
      if constexpr (XPV) ox[i] = mfma16(vx[i & 3], __builtin_bit_cast(s16x4, px), ox[i]);
      if constexpr (i < 6) ldx(XM_, I2{}, xo); else ldx(NXM_, I2{}, nxo);
      if constexpr (XSM && (i == 2 || i == 4)) {
        x0 = ex2(fmaf(sx[i - 2], CEXP, nmx)); x1 = ex2(fmaf(sx[i - 1], CEXP, nmx));
        asm volatile("" : "+v"(x0), "+v"(x1));
      }
      if constexpr (XSM && (i == 3 || i == 5)) {
        psx += x0 + x1; pxn[(i - 3) >> 1] = pk2(x0, x1);
        asm volatile("" : "+v"(pxn[(i - 3) >> 1]), "+v"(psx));
      }
      if constexpr (BAR >= 0 && i >= 6) { if (jd >= 0) issue_kv2(jd, d0, std::integral_constant<int, i - 6>{}); }
      if constexpr (XM == 1 && PV && i < 6) {
        if (jd >= 0) issue_kv2(jd + ((i + 2) >> 2), i < 2 ? d0 : d1, std::integral_constant<int, ((i + 2) & 3)>{});
      }
      SB();
    });
    if constexpr (SM) {
      ps += e0 + e1; pn[1][3] = pk2(e0, e1);
      if (__any(!(ps <= PSUM_LIMIT))) {
        const float mn = fmaxf(m, x32_max(max16(sc)));
        const float al = ex2((m - mn) * CEXP), nn = -mn * CEXP;
        m = mn; l *= al; ps = 0.f;
#pragma unroll
        for (int i = 0; i < 4; ++i) o[i] *= al;
#pragma unroll
        for (int i = 0; i < 8; ++i) {
          const float a0 = ex2(fmaf(sc[2 * i], CEXP, nn)), a1 = ex2(fmaf(sc[2 * i + 1], CEXP, nn));
          ps += a0 + a1;
          pn[i >> 2][i & 3] = pk2(a0, a1);
        }
      }
      l += ps;
    }
    if constexpr (XSM) {
      if (__any(!(psx <= PSUM_LIMIT))) {
        const float mn = fmaxf(mx, xg_max(fmaxf(fmaxf(sx[0], sx[1]), fmaxf(sx[2], sx[3]))));
        const float al = ex2((mx - mn) * CEXP), nn = -mn * CEXP;
        mx = mn; lx *= al;
#pragma unroll
        for (int i = 0; i < 8; ++i) ox[i] *= al;
        const float a0 = ex2(fmaf(sx[0], CEXP, nn)), a1 = ex2(fmaf(sx[1], CEXP, nn)), a2 = ex2(fmaf(sx[2], CEXP, nn)),
                    a3 = ex2(fmaf(sx[3], CEXP, nn));
        psx = (a0 + a1) + (a2 + a3);
        pxn[0] = pk2(a0, a1); pxn[1] = pk2(a2, a3);
      }
      lx += psx;
      px = pxn;
    }
  };
  using T = std::true_type; using F = std::false_type;
  using X0 = std::integral_constant<int, 0>; using X1 = std::integral_constant<int, 1>; using X2 = std::integral_constant<int, 2>;
  using X3 = std::integral_constant<int, 3>; using X4 = std::integral_constant<int, 4>; using NB_ = std::integral_constant<int, -1>;

  STAMP(2);
  m = 0.f; mx = 0.f;
  ldk(T{}, X0{}, 3 * SLOT); ldk(T{}, X1{}, 3 * SLOT); ldx(X1{}, X0{}, 3 * SLOT); ldx(X1{}, X1{}, 3 * SLOT);
  SB();
  stage(T{}, F{}, F{}, X1{}, T{}, F{}, X3{}, NB_{}, sA, sB, pB, pA, 3 * SLOT, 0, 3 * SLOT, 3 * SLOT + 8192, 0, 0, -1, 0, 0, false);
  m = x32_max(max16(sA));
  mx = xg_max(fmaxf(fmaxf(sx[0], sx[1]), fmaxf(sx[2], sx[3])));
  stage(T{}, F{}, T{}, X3{}, T{}, T{}, X1{}, X0{}, sB, sA, pB, pA, 3 * SLOT + 8192, 0, 0, 0, 4 * SLOT, 0, 4, 2 * SLOT, 0, false);
  unsigned cv = 4 * SLOT, ck = 0, d0 = 2 * SLOT, d1 = 3 * SLOT;
  asm volatile("" : "+v"(kb), "+v"(kbx), "+v"(vb), "+v"(vbx));
  for (int t = 0; t < 6; ++t) {
    unsigned nk = ck + 2 * SLOT, nv = cv + 2 * SLOT;
    if (nk >= NSLOT * SLOT) nk -= NSLOT * SLOT;
    if (nv >= NSLOT * SLOT) nv -= NSLOT * SLOT;
    stage(T{}, T{}, T{}, X1{}, T{}, T{}, X2{}, NB_{}, sA, sB, pA, pB, ck, cv, ck, ck + 8192, cv + 8192, cv, 2 * t + 4, d0, d1, false);
    stage(T{}, T{}, T{}, X2{}, T{}, T{}, X1{}, X1{}, sB, sA, pB, pA, ck + 8192, cv + 8192, cv, nk, nv, nk, t < 5 ? 2 * t + 6 : -1, cv, 0, false);
    d0 = cv; d1 = ck; ck = nk; cv = nv;
  }
  const int ln = __builtin_amdgcn_mbcnt_hi(~0u, __builtin_amdgcn_mbcnt_lo(~0u, 0u));
  const int hq = bid & 3, nt = sl & 3, half = bid >> 8, h0 = 8 * hq + half;
  const Gemm<DH * 2, 2 * SQ * DH * 2, DM * 2, 256, OX_POL, OW_POL> G(ws + (size_t)((p * NB + b) * NH + h0) * (SQ * DH * 2),
                                                        wo + (size_t)(h0 * DH) * (DM * 2) + nt * 256, lds0, w, ln, 2 * nt);
  G.issue_w4<0>();
  {
    const char* sb = wo + (size_t)(h * DH + b * 16 + w * 4) * (DM * 2);
    dma<0, 0, OW_POL>(ln * 16, sb, lkv); dma<1024, 1024, OW_POL>(ln * 16, sb, lkv); dma<2048, 2048, OW_POL>(ln * 16, sb, lkv);
    dma<3072, 3072, OW_POL>(ln * 16, sb, lkv);
  }
  asm volatile("" : "+v"(kb), "+v"(kbx), "+v"(vb), "+v"(vbx));
  stage(T{}, T{}, T{}, X1{}, T{}, T{}, X2{}, NB_{}, sA, sB, pA, pB, 2 * SLOT, SLOT, 2 * SLOT, 2 * SLOT + 8192, SLOT + 8192, SLOT, -1, 0, 0, false);
  stage(T{}, T{}, T{}, X2{}, F{}, T{}, X4{}, X1{}, sB, sA, pB, pA, 2 * SLOT + 8192, SLOT + 8192, SLOT, 0, 3 * SLOT, 3 * SLOT, -1, 0, 0, true);
  stage(F{}, T{}, T{}, X4{}, F{}, T{}, X0{}, NB_{}, sA, sB, pA, pB, 0, 3 * SLOT, 3 * SLOT, 0, 3 * SLOT + 8192, 0, -1, 0, 0, false);
  stage(F{}, T{}, F{}, X0{}, F{}, F{}, X0{}, NB_{}, sB, sA, pB, pA, 0, 3 * SLOT + 8192, 0, 0, 0, 0, -1, 0, 0, false);

  STAMP(4);
  const int g2 = ln >> 4, c2 = ln & 15;
  u64* flagO = (u64*)(ws + WS_FLAG);
  u64* flagP = flagO + 512 * 8;
  u64* flagS = flagP + 256 * 8;
  const auto rsO = __builtin_amdgcn_make_buffer_rsrc(ws, 0, WS_SLAB, 0x00020000);
  const auto rsS = __builtin_amdgcn_make_buffer_rsrc(ws + WS_SLAB, 0, WS_FLAG - WS_SLAB, 0x00020000);
  const unsigned ob = ((p * NB + b) * NH + h) * (SQ * DH * 2);
  lx = xg_sum(lx);
  wait_bar<0>();
  STAMPR(9);
  {
    const float rl = 1.f / x32_sum(l);
    const int r = ln & 31;
    const unsigned sw = lds0 + (4 * w + (r >> 3)) * 1024 + (r & 7) * 128;
#pragma unroll
    for (int db = 0; db < 4; ++db)
#pragma unroll
      for (int k = 0; k < 4; k += 2) {
        const unsigned a0 = pk2(o[db][4 * k] * rl, o[db][4 * k + 1] * rl), a1 = pk2(o[db][4 * k + 2] * rl, o[db][4 * k + 3] * rl);
        const unsigned b0 = pk2(o[db][4 * k + 4] * rl, o[db][4 * k + 5] * rl), b1 = pk2(o[db][4 * k + 6] * rl, o[db][4 * k + 7] * rl);
        auto r0 = __builtin_amdgcn_permlane32_swap(a0, b0, false, false);
        auto r1 = __builtin_amdgcn_permlane32_swap(a1, b1, false, false);
        *(LDS_AS u32x4*)(sw + XB[db >> 1] + (((4 * (db & 1) + k + (ln >> 5)) ^ ((r >> 1) & 7)) << 4)) = (u32x4){r0[0], r1[0], r0[1], r1[1]};
      }
#pragma unroll
    for (int dt = 0; dt < 8; ++dt)
      if ((dt >> 1) != w) *(LDS_AS f32x4*)(lds0 + SCR + (((dt >> 1) * 3 + ((w - (dt >> 1) - 1) & 3)) * 2 + (dt & 1)) * 1024 + ln * 16) = ox[dt];
    if (g2 == 0) { *(LDS_AS f32x2*)(lds0 + SCR + 24576 + w * 128 + c2 * 8) = (f32x2){mx, lx}; }
  }
  STAMPR(10);
  wait_bar<63>();
  STAMPR(11);
  {
    f32x2 ml[4];
#pragma unroll
    for (int i = 0; i < 4; ++i) ml[i] = *(const LDS_AS f32x2*)(lds0 + SCR + 24576 + i * 128 + c2 * 8);
    const float M = fmaxf(fmaxf(ml[0][0], ml[1][0]), fmaxf(ml[2][0], ml[3][0]));
    float L = 0.f;
#pragma unroll
    for (int i = 0; i < 4; ++i) L += ex2((ml[i][0] - M) * CEXP) * ml[i][1];
    const float rl = 1.f / L;
    f32x4 a0 = w == 0 ? ox[0] : w == 1 ? ox[2] : w == 2 ? ox[4] : ox[6], a1 = w == 0 ? ox[1] : w == 1 ? ox[3] : w == 2 ? ox[5] : ox[7];
    const float so = ex2((mx - M) * CEXP) * rl;
    a0 *= so; a1 *= so;
#pragma unroll
    for (int i = 0; i < 3; ++i) {
      const f32x2 mi = *(const LDS_AS f32x2*)(lds0 + SCR + 24576 + ((w + 1 + i) & 3) * 128 + c2 * 8);
      const float si = ex2((mi[0] - M) * CEXP) * rl;
      const unsigned pb = lds0 + SCR + (w * 3 + i) * 2048 + ln * 16;
      a0 += *(const LDS_AS f32x4*)(pb) * si;
      a1 += *(const LDS_AS f32x4*)(pb + 1024) * si;
    }
    const u32x4 v = c2frag(pk4(a0), pk4(a1));
    *(LDS_AS u32x4*)(lds0 + ((w >> 1) ? XB[1] : XB[0]) + (16 + (c2 >> 3)) * 1024 + (c2 & 7) * 128 + (((4 * (w & 1) + g2) ^ (c2 >> 1)) << 4)) = v;
    __builtin_amdgcn_raw_buffer_store_b128(v, rsO, ob + (128 + c2) * 256 + w * 64 + g2 * 16, 0, 16);
  }
  STAMP(5);
  if (half == 0) __builtin_amdgcn_s_setprio(0);
  f32x4 acc[9][2];
#pragma unroll
  for (int i = 0; i < 9; ++i) { acc[i][0] = f32x4{}; acc[i][1] = f32x4{}; }
  const unsigned foff = ln == 4 ? (512 + (bid & 255)) * 64 : (((p * NB + b) * NH + h0) + 2 * ((nt + ln) & 3)) * 64;
  u32x2 fl;
  auto fl_load = [&]() { gld_flag(fl, foff, (const char*)flagO); };
  auto fl_bit = [&](int k) {
    asm volatile("" : "+v"(fl));
    return (bool)((__ballot((((u64)fl[1] << 32) | fl[0]) == tag) >> k) & 1);
  };
  auto fl_spin = [&](int k) {
    const gu64* f = (const gu64*)((const char*)flagO + __builtin_amdgcn_readlane(foff, k));
    for (int spins = 0; spins < (1 << 17); ++spins) {
      if (__hip_atomic_load(f, __ATOMIC_RELAXED, __HIP_MEMORY_SCOPE_AGENT) == tag) break;
      __builtin_amdgcn_s_sleep(2);
    }
  };
  u32x4 pp[9][2];
  bool early = false;
  const unsigned pbo = (bid & 255) * 73728 + w * 2048 + ln * 16;
  auto pp_load = [&]() {
    static_for<9>([&](auto R) {
      constexpr int rt = decltype(R)::value;
      gld_sc1<0>(pp[rt][0], pbo + rt * 8192, ws + WS_SLAB); gld_sc1<1024>(pp[rt][1], pbo + rt * 8192, ws + WS_SLAB);
    });
  };
  wait_bar<63>();
  const char *sxb = nullptr, *swb = G.wbase(1);
  u32x4 ov;
  G.run(acc, [&](auto S, auto I) {
    constexpr int s = decltype(S)::value, i = decltype(I)::value;
    if constexpr (i == 0 && ((s & 1) == 0 || s == 5) && s < 6) fl_load();
    if constexpr (s == 0) {
      if constexpr (i < 4) G.template issue_w<1, i>(swb);
      if constexpr (i >= 5 && i < 13) {
        constexpr int k = i - 5;
        __builtin_amdgcn_raw_buffer_store_b128(
            ov, rsO, ob + (32 * w + 8 * (k & 3) + (ln >> 3)) * 256 + (k >> 2) * 128 + (((ln & 7) ^ ((4 * (k & 3) + g2) & 7)) << 4), 0, 16);
      }
      if constexpr (i >= 4 && i < 12) ov = *(const LDS_AS u32x4*)(lds0 + XB[(i - 4) >> 2] + (4 * w + ((i - 4) & 3)) * 1024 + ln * 16);
    }
    if constexpr (i == 15 && s < 7) {
      wait_bar<0>();
      if constexpr (s == 0) {
        if (threadIdx.x == 0)
          __hip_atomic_store((gu64*)(flagO + ((p * NB + b) * NH + h) * 8), tag, __ATOMIC_RELAXED, __HIP_MEMORY_SCOPE_AGENT);
        STAMPR(12);
      }
      if constexpr ((s & 1) == 0 && s < 6) { if (!fl_bit((s + 2) >> 1)) fl_spin((s + 2) >> 1); }
      if constexpr (s == 0) STAMPR(13);
      if constexpr (s == 1) STAMP(7);
      if constexpr (s == 3) STAMPR(14);
      if constexpr (s == 5) { const bool pr = fl_bit(4); early = pr && half; }
      if constexpr (s == 6) { if (early) pp_load(); }
      sxb = G.xbase(s + 2); swb = G.wbase(s + 2);
    }
    constexpr int t = i >= 15 ? s + 2 : s + 1, n = i >= 15 ? i - 15 : i + 3;
    if constexpr (n < 9 && (i >= 15 ? s < 7 : s >= 1) && t < 8) G.template issue<t, n>(swb, sxb);
  });
  STAMP(3);
  if (half == 0) {
#pragma unroll
    for (int rt = 0; rt < 9; ++rt) {
      __builtin_amdgcn_raw_buffer_store_b128(__builtin_bit_cast(u32x4, acc[rt][0]), rsS, pbo + rt * 8192, 0, 16);
      __builtin_amdgcn_raw_buffer_store_b128(__builtin_bit_cast(u32x4, acc[rt][1]), rsS, pbo + rt * 8192 + 1024, 0, 16);
    }
    publish(flagP + (bid & 255) * 8, tag);
  } else {
    if (!early) { fl_spin(4); pp_load(); }
    asm volatile("s_waitcnt vmcnt(0)"
                 : "+v"(pp[0][0]), "+v"(pp[0][1]), "+v"(pp[1][0]), "+v"(pp[1][1]), "+v"(pp[2][0]), "+v"(pp[2][1]), "+v"(pp[3][0]), "+v"(pp[3][1]),
                   "+v"(pp[4][0]), "+v"(pp[4][1]), "+v"(pp[5][0]), "+v"(pp[5][1]), "+v"(pp[6][0]), "+v"(pp[6][1]), "+v"(pp[7][0]), "+v"(pp[7][1]),
                   "+v"(pp[8][0]), "+v"(pp[8][1]));
    const int tile = (p * NB + b) * 4 + nt;
    const int own0 = hq == 0 ? 0 : 2 * hq + 1, own1 = 2 * hq + 3;
    const unsigned sb = (256u + tile * 4) * 73728 + w * 2048 + ln * 16;
#pragma unroll
    for (int rt = 0; rt < 9; ++rt) {
      acc[rt][0] = __builtin_bit_cast(f32x4, pp[rt][0]) + acc[rt][0];
      acc[rt][1] = __builtin_bit_cast(f32x4, pp[rt][1]) + acc[rt][1];
      if (rt < own0 || rt >= own1) {
        __builtin_amdgcn_raw_buffer_store_b128(__builtin_bit_cast(u32x4, acc[rt][0]), rsS, sb + hq * 73728 + rt * 8192, 0, 16);
        __builtin_amdgcn_raw_buffer_store_b128(__builtin_bit_cast(u32x4, acc[rt][1]), rsS, sb + hq * 73728 + rt * 8192 + 1024, 0, 16);
      }
    }
    publish(flagS + (tile * 4 + hq) * 8, tag);
    await(flagS + tile * 32, 8, 4, tag, w, ln, false);
    u16* orow = out + (size_t)(b * SQ + c2) * (2 * DM) + p * DM + nt * 128 + 32 * w + 8 * g2;
#pragma unroll
    for (int rt = 0; rt < 9; ++rt)
      if (rt >= own0 && rt < own1) {
        f32x4 t0[4], t1[4];
#pragma unroll
        for (int k = 0; k < 4; ++k) {
          const unsigned a = sb + (k == hq ? 0x7f000000u : k * 73728 + rt * 8192);
          t0[k] = __builtin_bit_cast(f32x4, __builtin_amdgcn_raw_buffer_load_b128(rsS, a, 0, 16));
          t1[k] = __builtin_bit_cast(f32x4, __builtin_amdgcn_raw_buffer_load_b128(rsS, a + 1024, 0, 16));
        }
        f32x4 s0 = f32x4{}, s1 = f32x4{};
#pragma unroll
        for (int k = 0; k < 4; ++k) {
          s0 += k == hq ? acc[rt][0] : t0[k];
          s1 += k == hq ? acc[rt][1] : t1[k];
        }
        *(u32x4*)(orow + (size_t)(16 * rt) * (2 * DM)) = c2frag(pk4(s0), pk4(s1));
      }
  }
  STAMP(1); STAMP_FLUSH(0);
}

extern "C" void kernel_launch(void* const* d_in, const int* in_sizes, int n_in, void* d_out, int out_size, void* d_ws, size_t ws_size,
                              hipStream_t stream) {
  (void)in_sizes; (void)n_in; (void)out_size; (void)ws_size;
  const u16* const* in = (const u16* const*)d_in;
  (void)hipFuncSetAttribute((const void*)mha_fused, hipFuncAttributeMaxDynamicSharedMemorySize, LDS_BYTES);
  mha_fused<<<512, 256, LDS_BYTES, stream>>>(in[0], in[1], in[2], in[3], in[4], in[5], in[6], in[7], in[8], in[9], (char*)d_ws,
                                             (u16*)d_out);
}
```

```cpp
#include <hip/hip_runtime.h>
#include <hip/hip_bf16.h>
#include <stdint.h>
#include <type_traits>

typedef unsigned short u16;
typedef __attribute__((ext_vector_type(8))) short s16x8;
typedef __attribute__((ext_vector_type(4))) short s16x4;
typedef __attribute__((ext_vector_type(16))) float f32x16;
typedef __attribute__((ext_vector_type(4))) float f32x4;
typedef __attribute__((ext_vector_type(2))) float f32x2;
typedef __attribute__((ext_vector_type(4))) unsigned u32x4;
typedef __attribute__((ext_vector_type(2))) unsigned u32x2;
typedef __attribute__((ext_vector_type(2))) __bf16 bf16x2_t;
#define LDS_AS __attribute__((address_space(3)))
#define DI __device__ __forceinline__
#define SB() __builtin_amdgcn_sched_barrier(0)

constexpr int NB = 8, SQ = 144, DM = 512, NH = 32, SK = 512, DH = 128, HD = NH * DH;
constexpr int SLOT = 16384, NSLOT = 5, LDS_BYTES = SLOT * NSLOT;
constexpr unsigned WS_SLAB = 2u * NB * NH * SQ * DH * 2, WS_FLAG = WS_SLAB + 64u * 8 * 73728;
constexpr float CEXP = 0.08838834764831845f * 1.4426950408889634f;
constexpr float PSUM_LIMIT = 4096.f;


template <int N, class F> DI void static_for(F&& f) {
  if constexpr (N > 0) { static_for<N - 1>(f); f(std::integral_constant<int, N - 1>{}); }
}

DI f32x16 mfmaL(s16x8 a, s16x8 b, f32x16 c) { return __builtin_amdgcn_mfma_f32_32x32x16_bf16(a, b, c, 0, 0, 0); }
DI f32x4 mfma32(s16x8 a, s16x8 b, f32x4 c) { return __builtin_amdgcn_mfma_f32_16x16x32_bf16(a, b, c, 0, 0, 0); }
DI f32x4 mfma16(s16x4 a, s16x4 b, f32x4 c) { return __builtin_amdgcn_mfma_f32_16x16x16bf16_1k(a, b, c, 0, 0, 0); }
DI s16x8 lds_b128(unsigned a) { return *(const LDS_AS s16x8*)(a); }
DI s16x4 lds_tr(unsigned a) { return __builtin_amdgcn_ds_read_tr16_b64_v4i16((LDS_AS s16x4*)(a)); }
DI s16x8 cat8(s16x4 lo, s16x4 hi) { return __builtin_shufflevector(lo, hi, 0, 1, 2, 3, 4, 5, 6, 7); }
DI unsigned pk2(float a, float b) { f32x2 v = {a, b}; return __builtin_bit_cast(unsigned, __builtin_convertvector(v, bf16x2_t)); }
DI u32x2 pk4(f32x4 v) { u32x2 r = {pk2(v[0], v[1]), pk2(v[2], v[3])}; return r; }
DI float ex2(float x) { return __builtin_amdgcn_exp2f(x); }

DI u32x4 c2frag(u32x2 t0, u32x2 t1) {
  u32x4 r;
#pragma unroll
  for (int i = 0; i < 2; ++i) {
    auto a = __builtin_amdgcn_permlane32_swap(t0[i], t1[i], false, false);
    auto b = __builtin_amdgcn_permlane16_swap(a[0], a[1], false, false);
    r[i] = b[0]; r[2 + i] = b[1];
  }
  return r;
}
DI float x32_max(float x) {
  auto b = __builtin_amdgcn_permlane32_swap(__float_as_uint(x), __float_as_uint(x), false, false);
  return fmaxf(__uint_as_float(b[0]), __uint_as_float(b[1]));
}
DI float x32_sum(float x) {
  auto b = __builtin_amdgcn_permlane32_swap(__float_as_uint(x), __float_as_uint(x), false, false);
  return __uint_as_float(b[0]) + __uint_as_float(b[1]);
}
DI float xg_max(float x) {
  auto a = __builtin_amdgcn_permlane16_swap(__float_as_uint(x), __float_as_uint(x), false, false);
  return x32_max(fmaxf(__uint_as_float(a[0]), __uint_as_float(a[1])));
}
DI float xg_sum(float x) {
  auto a = __builtin_amdgcn_permlane16_swap(__float_as_uint(x), __float_as_uint(x), false, false);
  return x32_sum(__uint_as_float(a[0]) + __uint_as_float(a[1]));
}
DI float max16(const f32x16& s) {
  float a = fmaxf(fmaxf(s[0], s[1]), s[2]), b = fmaxf(fmaxf(s[3], s[4]), s[5]);
  a = fmaxf(fmaxf(a, s[6]), s[7]); b = fmaxf(fmaxf(b, s[8]), s[9]);
  a = fmaxf(fmaxf(a, s[10]), s[11]); b = fmaxf(fmaxf(b, s[12]), s[13]);
  return fmaxf(fmaxf(a, s[14]), fmaxf(b, s[15]));
}

template <int GOFF, int LOFF, int POL>
DI void dma(unsigned voff, const char* sb, unsigned lb) {
  if constexpr (POL == 1)
    asm volatile("s_add_u32 m0, %2, %3\n\ts_nop 0\n\tglobal_load_lds_dwordx4 %0, %1 offset:%4 nt"
                 ::"v"(voff), "s"(sb), "s"(lb), "i"(LOFF - GOFF), "i"(GOFF) : "memory", "scc", "m0");
  else if constexpr (POL == 2)
    asm volatile("s_add_u32 m0, %2, %3\n\ts_nop 0\n\tglobal_load_lds_dwordx4 %0, %1 offset:%4 sc1"
                 ::"v"(voff), "s"(sb), "s"(lb), "i"(LOFF - GOFF), "i"(GOFF) : "memory", "scc", "m0");
  else
    asm volatile("s_add_u32 m0, %2, %3\n\ts_nop 0\n\tglobal_load_lds_dwordx4 %0, %1 offset:%4"
                 ::"v"(voff), "s"(sb), "s"(lb), "i"(LOFF - GOFF), "i"(GOFF) : "memory", "scc", "m0");
}
typedef unsigned long long u64;
typedef __attribute__((address_space(1))) u64 gu64;
extern "C" __device__ u64 hw_dispatch_id() __asm("llvm.amdgcn.dispatch.id");
DI void publish(u64* flag, u64 tag) {
  asm volatile("s_waitcnt vmcnt(0) lgkmcnt(0)\n\ts_barrier" ::: "memory");
  if (threadIdx.x == 0) __hip_atomic_store((gu64*)flag, tag, __ATOMIC_RELAXED, __HIP_MEMORY_SCOPE_AGENT);
}
DI void await(const u64* flags, int stride, int n, u64 tag, int w, int lane) {
  if (w == 0) {
    const gu64* f = (const gu64*)flags + (lane < n ? lane : 0) * stride;
    for (int spins = 0; spins < (1 << 17); ++spins) {
      if (__all(__hip_atomic_load(f, __ATOMIC_RELAXED, __HIP_MEMORY_SCOPE_AGENT) == tag)) break;
      __builtin_amdgcn_s_sleep(2);
    }
  }
  asm volatile("s_waitcnt vmcnt(0) lgkmcnt(0)\n\ts_barrier" ::: "memory");
}
template <int OFF> DI void gld_sc1(u32x4& d, unsigned voff, const char* sb) {
  asm volatile("global_load_dwordx4 %0, %1, %2 offset:%3 sc1" : "=v"(d) : "v"(voff), "s"(sb), "i"(OFF));
}
DI void gld_flag(u32x2& d, unsigned voff, const char* sb) {
  asm volatile("global_load_dwordx2 %0, %1, %2 sc1" : "=v"(d) : "v"(voff), "s"(sb));
}
template <int N> DI void wait_bar() { asm volatile("s_waitcnt vmcnt(%0) lgkmcnt(0)\n\ts_barrier" ::"i"(N) : "memory"); }
template <int N> DI void wait_vm() { asm volatile("s_waitcnt vmcnt(%0)" ::"i"(N) : "memory"); }

DI int tsw(int r) { return (r & 2) | ((r >> 2) & 1); }
constexpr int XB[2] = {47104, 0}, WB[2] = {4 * SLOT, 18432}, SCR = 18432;

template <int AST, int ABLK, int WST, int WBLK, int XPOL, int WPOL>
struct Gemm {
  unsigned va[5], vw[2], lw, lx, lx4, bx0, bx1, bw0, bw1;
  const char *ag, *wsrc;
  int rot;
  bool x5;
  DI Gemm(const char* ag_, const char* wsrc_, unsigned lds0, int w, int lane, int rot_) : ag(ag_), wsrc(wsrc_), rot(rot_) {
    const int g = lane >> 4, c = lane & 15, pos = lane >> 3, row = (lane >> 2) & 7;
    const unsigned va0 = ((lane >> 3) * AST + (((lane & 7) ^ (lane >> 4)) << 4)) ^ ((w & 1) << 6);
#pragma unroll
    for (int i = 0; i < 4; ++i) va[i] = va0 + (8 * w + 32 * i) * AST;
    va[4] = va0 + (128 + 8 * (w & 1)) * AST;
    vw[0] = (16 * w + (pos & 3) + 8 * (pos >> 2)) * WST + (((lane & 7) ^ (tsw(pos) << 1)) << 4); vw[1] = vw[0] + 4 * WST;
    x5 = w < 2;
    lw = lds0 + w * 4096; lx = lds0 + w * 1024; lx4 = lds0 + 16384 + (w & 1) * 1024;
    bx0 = lds0 + (c >> 3) * 1024 + (c & 7) * 128 + ((g ^ (c >> 1)) << 4); bx1 = bx0 ^ 64;
    bw0 = lds0 + (g >> 1) * 4096 + (w >> 1) * 1024 + row * 128 + (((2 * (w & 1)) ^ tsw(row)) << 5) + (lane & 3) * 8; bw1 = bw0 ^ 32;
  }
  DI const char* wbase(int t) const { const int tr = (t + rot) & 7; return wsrc + (size_t)((tr >> 1) * WBLK + (tr & 1) * 64) * WST; }
  DI const char* xbase(int t) const { const int tr = (t + rot) & 7; return ag + (tr >> 1) * ABLK + (tr & 1) * 128; }
  template <int T, int I> DI void issue_w(const char* sb) const { dma<(I & 1) * 128, WB[T & 1] + I * 1024, WPOL>(vw[I >> 1], sb, lw); }
  template <int T, int I> DI void issue_x(const char* sb) const {
    if constexpr (I < 4) dma<0, XB[T & 1] + I * 4096, XPOL>(va[I], sb, lx);
    else { if (x5) dma<0, XB[T & 1], XPOL>(va[4], sb, lx4); }
  }
  template <int T> DI void issue_w4() const {
    const char* sb = wbase(T);
    issue_w<T, 0>(sb); issue_w<T, 1>(sb); issue_w<T, 2>(sb); issue_w<T, 3>(sb);
  }
  template <int T> DI void issue_x5() const {
    const char* sb = xbase(T);
    issue_x<T, 0>(sb); issue_x<T, 1>(sb); issue_x<T, 2>(sb); issue_x<T, 3>(sb); issue_x<T, 4>(sb);
  }
  template <int T, int N> DI void issue(const char* swb, const char* sxb) const {
    if constexpr (N < 4) issue_w<T, N>(swb); else issue_x<T, N - 4>(sxb);
  }
  template <class Hook> DI void run(f32x4 (&acc)[9][2], Hook&& hook) const {
    s16x8 xf[4], wf[2][2][2];
    auto ldx = [&](auto N) {
      constexpr int n = decltype(N)::value, s = n / 18, i = n % 18;
      if constexpr (n < 144) xf[n & 3] = lds_b128((i >= 9 ? bx1 : bx0) + XB[s & 1] + (i % 9) * 2048);
    };
    auto ldw = [&](auto S, auto KS) {
      constexpr int s = decltype(S)::value, ks = decltype(KS)::value, wo = WB[s & 1] + ks * 8192;
      wf[s & 1][ks][0] = cat8(lds_tr(bw0 + wo), lds_tr(bw0 + wo + 2048));
      wf[s & 1][ks][1] = cat8(lds_tr(bw1 + wo), lds_tr(bw1 + wo + 2048));
    };
    using Z = std::integral_constant<int, 0>; using O1 = std::integral_constant<int, 1>;
    ldw(Z{}, Z{});
    static_for<3>(ldx);
    SB();
    static_for<144>([&](auto N) {
      constexpr int n = decltype(N)::value, s = n / 18, i = n % 18;
      hook(std::integral_constant<int, s>{}, std::integral_constant<int, i>{});
      if constexpr (i == 15 && s < 7) ldw(std::integral_constant<int, s + 1>{}, Z{});
      if constexpr (i == 3) ldw(std::integral_constant<int, s>{}, O1{});
      ldx(std::integral_constant<int, n + 3>{});
      acc[i % 9][0] = mfma32(wf[s & 1][i / 9][0], xf[n & 3], acc[i % 9][0]);
      acc[i % 9][1] = mfma32(wf[s & 1][i / 9][1], xf[n & 3], acc[i % 9][1]);
      asm volatile("" : "+v"(acc[i % 9][0]), "+v"(acc[i % 9][1]));
      SB();
    });
  }
};

__global__ __launch_bounds__(256, 2) void mha_fused(const u16* __restrict__ xA, const u16* __restrict__ kA, const u16* __restrict__ vA,
                                                    const u16* __restrict__ wqA, const u16* __restrict__ woA, const u16* __restrict__ xB,
                                                    const u16* __restrict__ kB, const u16* __restrict__ vB, const u16* __restrict__ wqB,
                                                    const u16* __restrict__ woB, char* ws, u16* out) {
  extern __shared__ __attribute__((aligned(128))) char smem[];
  const unsigned lds0 = (unsigned)(uintptr_t)(LDS_AS char*)smem;
  const int lane = threadIdx.x & 63;
  const int w = __builtin_amdgcn_readfirstlane(threadIdx.x >> 6);
  const int g = lane >> 4, c = lane & 15, r32 = lane & 31, hh = lane >> 5;
  const int bid = blockIdx.x, p = (bid >> 2) & 1, sl = (bid >> 3) & 31;
  const int h = (bid & 3) * 8 + 2 * (sl & 3) + (bid >> 8), b = sl >> 2;
  const char* xg = (const char*)(p ? xB : xA) + (size_t)b * SQ * DM * 2;
  const char* wq = (const char*)(p ? wqB : wqA) + h * (DH * 2);
  const char* kg = (const char*)(p ? kB : kA) + (size_t)(b * NH + h) * SK * DH * 2 + w * 4096;
  const char* vg = (const char*)(p ? vB : vA) + (size_t)(b * NH + h) * SK * DH * 2 + w * 4096;
  const char* wo = (const char*)(p ? woB : woA);
  u64 tag = (hw_dispatch_id() + 1) * 0x9E3779B97F4A7C15ull ^ (u64)(uintptr_t)__builtin_amdgcn_dispatch_ptr() * 0xC2B2AE3D27D4EB4Full;
  asm volatile("" : "+s"(wo), "+s"(ws), "+s"(out), "+s"(tag), "+s"(kg), "+s"(vg));
  const unsigned vkE = (lane >> 3) * 256 + (((lane & 7) ^ (lane >> 4)) << 4), vkO = vkE ^ 64;
  const unsigned vv = (lane >> 3) * 256 + (((lane & 7) ^ (tsw(lane >> 3) << 1)) << 4);
  const unsigned lkv = lds0 + w * 4096;
  auto issue_kv2 = [&](int j, unsigned so, auto I) {
    constexpr int i = decltype(I)::value;
    const char* s = ((j & 1) ? vg : kg) + (j >> 1) * SLOT;
    dma<(i & 1) * 128 + (i >> 1) * 2048, i * 1024, 1>((j & 1) ? vv : ((i >> 1) ? vkO : vkE), s, lkv + so);
  };
  auto issue_kv = [&](int j, auto I) { issue_kv2(j, ((j + 3) % NSLOT) * SLOT, I); };
  unsigned kb = lds0 + (r32 >> 3) * 2048 + (r32 & 7) * 128 + ((hh ^ ((r32 >> 1) & 7)) << 4);
  unsigned kbx = lds0 + w * 4096 + (c >> 3) * 2048 + (c & 7) * 128 + ((g ^ (c >> 1)) << 4);

  __builtin_amdgcn_s_setprio(1);
  s16x8 q[8], qx[4];
  {
    f32x4 acc[9][2];
#pragma unroll
    for (int i = 0; i < 9; ++i) { acc[i][0] = f32x4{}; acc[i][1] = f32x4{}; }
    const Gemm<DM * 2, 256, HD * 2, 128, 0, 0> G(xg, wq, lds0, w, lane, b + h);
    G.issue_w4<0>(); G.issue_x5<0>(); G.issue_w4<1>(); G.issue_x5<1>();
    wait_bar<8>();
    const char *sxb = nullptr, *swb = nullptr;
    G.run(acc, [&](auto S, auto I) {
      constexpr int s = decltype(S)::value, i = decltype(I)::value;
      if constexpr (i == 15 && s < 7) { wait_bar<0>(); sxb = G.xbase(s + 2); swb = G.wbase(s + 2); }
      constexpr int t = i >= 15 ? s + 2 : s + 1, n = i >= 15 ? i - 15 : i + 3;
      if constexpr (n < 9 && (i >= 15 ? s < 7 : s >= 1)) {
        if constexpr (t < 8) G.template issue<t, n>(swb, sxb);
        else if constexpr (n < 8) issue_kv(n >> 2, std::integral_constant<int, (n & 3)>{});
      }
    });
    const unsigned qw = lds0 + SLOT + (c >> 3) * 2048 + (w >> 1) * 1024 + (c & 7) * 128 + (((4 * (w & 1) + g) ^ (c >> 1)) << 4);
    wait_bar<63>();
    static_for<4>([&](auto I) { issue_kv(2, I); });
#pragma unroll
    for (int rt = 0; rt < 8; ++rt) *(LDS_AS u32x4*)(qw + rt * 4096) = c2frag(pk4(acc[rt][0]), pk4(acc[rt][1]));
    const u32x4 fx = c2frag(pk4(acc[8][0]), pk4(acc[8][1]));
    wait_bar<63>();
#pragma unroll
    for (int ks = 0; ks < 8; ++ks) q[ks] = lds_b128((kb ^ ((ks & 3) << 5)) + SLOT + w * 8192 + (ks >> 2) * 1024);
    asm volatile("s_waitcnt lgkmcnt(0)" : "+v"(q[0]), "+v"(q[1]), "+v"(q[2]), "+v"(q[3]), "+v"(q[4]), "+v"(q[5]), "+v"(q[6]), "+v"(q[7])::"memory");
    *(LDS_AS u32x4*)(lds0 + SLOT + w * 8192 + lane * 16) = fx;
    wait_bar<8>();
#pragma unroll
    for (int ks = 0; ks < 4; ++ks) qx[ks] = lds_b128(lds0 + SLOT + ks * 8192 + lane * 16);
  }

  f32x16 o[4];
  f32x4 ox[8];
#pragma unroll
  for (int i = 0; i < 4; ++i) o[i] = f32x16{};
#pragma unroll
  for (int i = 0; i < 8; ++i) ox[i] = f32x4{};
  float m, mx, l = 0.f, lx = 0.f;
  f32x16 sA, sB;
  f32x4 sx;
  u32x4 pA[2], pB[2];
  u32x2 px;
  const int vr = 4 * hh + ((lane >> 2) & 3);
  unsigned vb = lds0 + vr * 128 + (((g & 1) ^ tsw(vr)) << 5) + (lane & 3) * 8;
  const int vrx = (lane >> 2) & 7;
  unsigned vbx = lds0 + w * 4096 + (g >> 1) * 2048 + vrx * 128 + (tsw(vrx) << 5) + (lane & 3) * 8;

  s16x8 kf[4], vf[4], kx[4];
  s16x4 vx[4];
  auto ldk = [&](auto QK_, auto I, unsigned ko) {
    constexpr int i = decltype(I)::value;
    if constexpr (decltype(QK_)::value) kf[i & 3] = lds_b128(((kb + ko) ^ ((i & 3) << 5)) + (i >> 2) * 1024);
  };
  auto ldv = [&](auto PV_, auto I, unsigned vo) {
    constexpr int i = decltype(I)::value;
    if constexpr (decltype(PV_)::value) {
      const unsigned a = ((vb + vo) ^ (((i >> 1) & 1) << 6)) + (i & 1) * 4096 + (i >> 2) * 1024;
      vf[i & 3] = cat8(lds_tr(a), lds_tr(a + 2048));
    }
  };
  auto ldx = [&](auto XM_, auto I, unsigned xo) {
    constexpr int XM = decltype(XM_)::value, i = decltype(I)::value;
    if constexpr (XM == 1 && i < 4) kx[i] = lds_b128(((kbx + xo) ^ ((i & 1) << 6)) + (i >> 1) * 1024);
    if constexpr (XM == 2 || XM == 4) vx[i & 3] = lds_tr(((vbx + xo) ^ ((i & 3) << 5)) + (i >> 2) * 1024);
  };
  auto stage = [&](auto QK_, auto PV_, auto SM_, auto XM_, auto NQK_, auto NPV_, auto NXM_, auto BAR_, f32x16& sn, const f32x16& sc,
                   const u32x4 (&pc)[2], u32x4 (&pn)[2], unsigned ko, unsigned vo, unsigned xo, unsigned nko, unsigned nvo, unsigned nxo,
                   int jd, unsigned d0, unsigned d1, bool last, int jp = -1) {
    constexpr bool QK = decltype(QK_)::value, PV = decltype(PV_)::value, SM = decltype(SM_)::value;
    constexpr int XM = decltype(XM_)::value, BAR = decltype(BAR_)::value;
    constexpr bool XPV = XM == 2 || XM == 4, XSM = XM == 2 || XM == 3;
    const float nm = -m * CEXP, nmx = -mx * CEXP;
    float ps = 0.f, psx = 0.f, e0 = 0.f, e1 = 0.f, x0 = 0.f, x1 = 0.f;
    u32x2 pxn;
    static_for<8>([&](auto I) {
      constexpr int i = decltype(I)::value;
      if constexpr (i == 6 && BAR >= 0) {
        if constexpr (BAR == 0) { wait_bar<0>(); static_for<4>([&](auto J) { issue_kv(3, J); }); }
        else { if (last) wait_bar<0>(); else wait_bar<4>(); }
      }
      {
        constexpr bool prevN = (i + 1) >= 8;
        constexpr int j = (i + 1) & 7;
        constexpr bool qk1 = prevN ? decltype(NQK_)::value : QK, pv1 = prevN ? decltype(NPV_)::value : PV;
        constexpr int xm1 = prevN ? decltype(NXM_)::value : XM;
        constexpr int n1 = (qk1 ? 1 : 0) + (pv1 ? 2 : 0) + ((xm1 == 1 && j < 4) || xm1 == 2 || xm1 == 4 ? 1 : 0);
        __builtin_amdgcn_s_waitcnt(0xC07F | (n1 << 8));
      }
      if constexpr (QK) sn = mfmaL(kf[i & 3], q[i], i == 0 ? f32x16{} : sn);
      using I2 = std::integral_constant<int, (i + 2) & 7>;
      if constexpr (i < 6) ldk(QK_, I2{}, ko); else ldk(NQK_, I2{}, nko);
      float f0, f1;
      if constexpr (SM) {
        f0 = fmaf(sc[2 * i], CEXP, nm); f1 = fmaf(sc[2 * i + 1], CEXP, nm);
        if constexpr (i > 0) { ps += e0 + e1; pn[(i - 1) >> 2][(i - 1) & 3] = pk2(e0, e1); asm volatile("" : "+v"(pn[(i - 1) >> 2][(i - 1) & 3]), "+v"(ps)); }
        asm volatile("" : "+v"(f0), "+v"(f1));
      }
      SB();
      if constexpr (PV) o[i >> 1] = mfmaL(vf[i & 3], __builtin_bit_cast(s16x8, pc[i & 1]), o[i >> 1]);
      if constexpr (i < 6) ldv(PV_, I2{}, vo); else ldv(NPV_, I2{}, nvo);
      if constexpr (SM) { e0 = ex2(f0); e1 = ex2(f1); asm volatile("" : "+v"(e0), "+v"(e1)); }
      SB();
      if constexpr (XM == 1 && i < 4) sx = mfma32(kx[i], qx[i], i == 0 ? f32x4{} : sx);
      if constexpr (XPV) ox[i] = mfma16(vx[i & 3], __builtin_bit_cast(s16x4, px), ox[i]);
      if constexpr (i < 6) ldx(XM_, I2{}, xo); else ldx(NXM_, I2{}, nxo);
      if constexpr (XSM && (i == 2 || i == 4)) {
        x0 = ex2(fmaf(sx[i - 2], CEXP, nmx)); x1 = ex2(fmaf(sx[i - 1], CEXP, nmx));
        asm volatile("" : "+v"(x0), "+v"(x1));
      }
      if constexpr (XSM && (i == 3 || i == 5)) {
        psx += x0 + x1; pxn[(i - 3) >> 1] = pk2(x0, x1);
        asm volatile("" : "+v"(pxn[(i - 3) >> 1]), "+v"(psx));
      }
      if constexpr (BAR >= 0 && i >= 6) { if (jd >= 0) issue_kv2(jd, d0, std::integral_constant<int, i - 6>{}); }
      if constexpr (XM == 1 && PV && i < 2) { if (jd >= 0) issue_kv2(jd, d0, std::integral_constant<int, i + 2>{}); }
      if constexpr (XM == 1 && PV && (i == 3 || i == 5 || i == 7)) { if (jd >= 0) issue_kv2(jd + 1, d1, std::integral_constant<int, (i - 3) / 2>{}); }
      if constexpr (XM == 2 && BAR == 1 && i == 2) { if (jp >= 0) issue_kv2(jp, d1, std::integral_constant<int, 3>{}); }
      SB();
    });
    if constexpr (SM) {
      ps += e0 + e1; pn[1][3] = pk2(e0, e1);
      if (__any(!(ps <= PSUM_LIMIT))) {
        const float mn = fmaxf(m, x32_max(max16(sc)));
        const float al = ex2((m - mn) * CEXP), nn = -mn * CEXP;
        m = mn; l *= al; ps = 0.f;
#pragma unroll
        for (int i = 0; i < 4; ++i) o[i] *= al;
#pragma unroll
        for (int i = 0; i < 8; ++i) {
          const float a0 = ex2(fmaf(sc[2 * i], CEXP, nn)), a1 = ex2(fmaf(sc[2 * i + 1], CEXP, nn));
          ps += a0 + a1;
          pn[i >> 2][i & 3] = pk2(a0, a1);
        }
      }
      l += ps;
    }
    if constexpr (XSM) {
      if (__any(!(psx <= PSUM_LIMIT))) {
        const float mn = fmaxf(mx, xg_max(fmaxf(fmaxf(sx[0], sx[1]), fmaxf(sx[2], sx[3]))));
        const float al = ex2((mx - mn) * CEXP), nn = -mn * CEXP;
        mx = mn; lx *= al;
#pragma unroll
        for (int i = 0; i < 8; ++i) ox[i] *= al;
        const float a0 = ex2(fmaf(sx[0], CEXP, nn)), a1 = ex2(fmaf(sx[1], CEXP, nn)), a2 = ex2(fmaf(sx[2], CEXP, nn)),
                    a3 = ex2(fmaf(sx[3], CEXP, nn));
        psx = (a0 + a1) + (a2 + a3);
        pxn[0] = pk2(a0, a1); pxn[1] = pk2(a2, a3);
      }
      lx += psx;
      px = pxn;
    }
  };
  using T = std::true_type; using F = std::false_type;
  using X0 = std::integral_constant<int, 0>; using X1 = std::integral_constant<int, 1>; using X2 = std::integral_constant<int, 2>;
  using X3 = std::integral_constant<int, 3>; using X4 = std::integral_constant<int, 4>; using NB_ = std::integral_constant<int, -1>;

  m = 0.f; mx = 0.f;
  ldk(T{}, X0{}, 3 * SLOT); ldk(T{}, X1{}, 3 * SLOT); ldx(X1{}, X0{}, 3 * SLOT); ldx(X1{}, X1{}, 3 * SLOT);
  SB();
  stage(T{}, F{}, F{}, X1{}, T{}, F{}, X3{}, NB_{}, sA, sB, pB, pA, 3 * SLOT, 0, 3 * SLOT, 3 * SLOT + 8192, 0, 0, -1, 0, 0, false);
  m = x32_max(max16(sA));
  mx = xg_max(fmaxf(fmaxf(sx[0], sx[1]), fmaxf(sx[2], sx[3])));
  stage(T{}, F{}, T{}, X3{}, T{}, T{}, X1{}, X0{}, sB, sA, pB, pA, 3 * SLOT + 8192, 0, 0, 0, 4 * SLOT, 0, 4, 2 * SLOT, 0, false);
  unsigned cv = 4 * SLOT, ck = 0, d0 = 2 * SLOT, d1 = 3 * SLOT;
  asm volatile("" : "+v"(kb), "+v"(kbx), "+v"(vb), "+v"(vbx));
  for (int t = 0; t < 6; ++t) {
    unsigned nk = ck + 2 * SLOT, nv = cv + 2 * SLOT;
    if (nk >= NSLOT * SLOT) nk -= NSLOT * SLOT;
    if (nv >= NSLOT * SLOT) nv -= NSLOT * SLOT;
    stage(T{}, T{}, T{}, X1{}, T{}, T{}, X2{}, NB_{}, sA, sB, pA, pB, ck, cv, ck, ck + 8192, cv + 8192, cv, 2 * t + 4, d0, d1, false);
    stage(T{}, T{}, T{}, X2{}, T{}, T{}, X1{}, X1{}, sB, sA, pB, pA, ck + 8192, cv + 8192, cv, nk, nv, nk, t < 5 ? 2 * t + 6 : -1, cv, d1, false, 2 * t + 5);
    d0 = cv; d1 = ck; ck = nk; cv = nv;
  }
  const int ln = __builtin_amdgcn_mbcnt_hi(~0u, __builtin_amdgcn_mbcnt_lo(~0u, 0u));
  const int hq = bid & 3, nt = sl & 3, half = bid >> 8, h0 = 8 * hq + half;
  const Gemm<DH * 2, 2 * SQ * DH * 2, DM * 2, 256, 2, 0> G(ws + (size_t)((p * NB + b) * NH + h0) * (SQ * DH * 2),
                                                        wo + (size_t)(h0 * DH) * (DM * 2) + nt * 256, lds0, w, ln, 2 * nt);
  G.issue_w4<0>();
  {
    const char* sb = wo + (size_t)(h * DH + b * 16 + w * 4) * (DM * 2);
    dma<0, 0, 0>(ln * 16, sb, lkv); dma<1024, 1024, 0>(ln * 16, sb, lkv); dma<2048, 2048, 0>(ln * 16, sb, lkv);
    dma<3072, 3072, 0>(ln * 16, sb, lkv);
  }
  asm volatile("" : "+v"(kb), "+v"(kbx), "+v"(vb), "+v"(vbx));
  stage(T{}, T{}, T{}, X1{}, T{}, T{}, X2{}, NB_{}, sA, sB, pA, pB, 2 * SLOT, SLOT, 2 * SLOT, 2 * SLOT + 8192, SLOT + 8192, SLOT, -1, 0, 0, false);
  stage(T{}, T{}, T{}, X2{}, F{}, T{}, X4{}, X1{}, sB, sA, pB, pA, 2 * SLOT + 8192, SLOT + 8192, SLOT, 0, 3 * SLOT, 3 * SLOT, -1, 0, 0, true);
  stage(F{}, T{}, T{}, X4{}, F{}, T{}, X0{}, NB_{}, sA, sB, pA, pB, 0, 3 * SLOT, 3 * SLOT, 0, 3 * SLOT + 8192, 0, -1, 0, 0, false);
  stage(F{}, T{}, F{}, X0{}, F{}, F{}, X0{}, NB_{}, sB, sA, pB, pA, 0, 3 * SLOT + 8192, 0, 0, 0, 0, -1, 0, 0, false);
  const int g2 = ln >> 4, c2 = ln & 15;
  u64* flagO = (u64*)(ws + WS_FLAG);
  u64* flagP = flagO + 512 * 8;
  u64* flagS = flagP + 256 * 8;
  const auto rsO = __builtin_amdgcn_make_buffer_rsrc(ws, 0, WS_SLAB, 0x00020000);
  const auto rsS = __builtin_amdgcn_make_buffer_rsrc(ws + WS_SLAB, 0, WS_FLAG - WS_SLAB, 0x00020000);
  const unsigned ob = ((p * NB + b) * NH + h) * (SQ * DH * 2);
  lx = xg_sum(lx);
  wait_bar<0>();
  {
    const float rl = 1.f / x32_sum(l);
    const int r = ln & 31;
    const unsigned sw = lds0 + (4 * w + (r >> 3)) * 1024 + (r & 7) * 128;
#pragma unroll
    for (int db = 0; db < 4; ++db)
#pragma unroll
      for (int k = 0; k < 4; k += 2) {
        const unsigned a0 = pk2(o[db][4 * k] * rl, o[db][4 * k + 1] * rl), a1 = pk2(o[db][4 * k + 2] * rl, o[db][4 * k + 3] * rl);
        const unsigned b0 = pk2(o[db][4 * k + 4] * rl, o[db][4 * k + 5] * rl), b1 = pk2(o[db][4 * k + 6] * rl, o[db][4 * k + 7] * rl);
        auto r0 = __builtin_amdgcn_permlane32_swap(a0, b0, false, false);
        auto r1 = __builtin_amdgcn_permlane32_swap(a1, b1, false, false);
        *(LDS_AS u32x4*)(sw + XB[db >> 1] + (((4 * (db & 1) + k + (ln >> 5)) ^ ((r >> 1) & 7)) << 4)) = (u32x4){r0[0], r1[0], r0[1], r1[1]};
      }
#pragma unroll
    for (int dt = 0; dt < 8; ++dt)
      if ((dt >> 1) != w) *(LDS_AS f32x4*)(lds0 + SCR + (((dt >> 1) * 3 + ((w - (dt >> 1) - 1) & 3)) * 2 + (dt & 1)) * 1024 + ln * 16) = ox[dt];
    if (g2 == 0) { *(LDS_AS f32x2*)(lds0 + SCR + 24576 + w * 128 + c2 * 8) = (f32x2){mx, lx}; }
  }
  wait_bar<63>();
  {
    f32x2 ml[4];
#pragma unroll
    for (int i = 0; i < 4; ++i) ml[i] = *(const LDS_AS f32x2*)(lds0 + SCR + 24576 + i * 128 + c2 * 8);
    const float M = fmaxf(fmaxf(ml[0][0], ml[1][0]), fmaxf(ml[2][0], ml[3][0]));
    float L = 0.f;
#pragma unroll
    for (int i = 0; i < 4; ++i) L += ex2((ml[i][0] - M) * CEXP) * ml[i][1];
    const float rl = 1.f / L;
    f32x4 a0 = w == 0 ? ox[0] : w == 1 ? ox[2] : w == 2 ? ox[4] : ox[6], a1 = w == 0 ? ox[1] : w == 1 ? ox[3] : w == 2 ? ox[5] : ox[7];
    const float so = ex2((mx - M) * CEXP) * rl;
    a0 *= so; a1 *= so;
#pragma unroll
    for (int i = 0; i < 3; ++i) {
      const f32x2 mi = *(const LDS_AS f32x2*)(lds0 + SCR + 24576 + ((w + 1 + i) & 3) * 128 + c2 * 8);
      const float si = ex2((mi[0] - M) * CEXP) * rl;
      const unsigned pb = lds0 + SCR + (w * 3 + i) * 2048 + ln * 16;
      a0 += *(const LDS_AS f32x4*)(pb) * si;
      a1 += *(const LDS_AS f32x4*)(pb + 1024) * si;
    }
    const u32x4 v = c2frag(pk4(a0), pk4(a1));
    *(LDS_AS u32x4*)(lds0 + ((w >> 1) ? XB[1] : XB[0]) + (16 + (c2 >> 3)) * 1024 + (c2 & 7) * 128 + (((4 * (w & 1) + g2) ^ (c2 >> 1)) << 4)) = v;
    __builtin_amdgcn_raw_buffer_store_b128(v, rsO, ob + (128 + c2) * 256 + w * 64 + g2 * 16, 0, 16);
  }
  if (half == 0) __builtin_amdgcn_s_setprio(0);
  f32x4 acc[9][2];
#pragma unroll
  for (int i = 0; i < 9; ++i) { acc[i][0] = f32x4{}; acc[i][1] = f32x4{}; }
  const unsigned foff = ln == 4 ? (512 + (bid & 255)) * 64 : (((p * NB + b) * NH + h0) + 2 * ((nt + ln) & 3)) * 64;
  u32x2 fl;
  auto fl_load = [&]() { gld_flag(fl, foff, (const char*)flagO); };
  auto fl_bit = [&](int k) {
    asm volatile("" : "+v"(fl));
    return (bool)((__ballot((((u64)fl[1] << 32) | fl[0]) == tag) >> k) & 1);
  };
  auto fl_spin = [&](int k) {
    const gu64* f = (const gu64*)((const char*)flagO + __builtin_amdgcn_readlane(foff, k));
    for (int spins = 0; spins < (1 << 17); ++spins) {
      if (__hip_atomic_load(f, __ATOMIC_RELAXED, __HIP_MEMORY_SCOPE_AGENT) == tag) break;
      __builtin_amdgcn_s_sleep(2);
    }
  };
  u32x4 pp[9][2];
  bool early = false;
  const unsigned pbo = (bid & 255) * 73728 + w * 2048 + ln * 16;
  auto pp_load = [&]() {
    static_for<9>([&](auto R) {
      constexpr int rt = decltype(R)::value;
      gld_sc1<0>(pp[rt][0], pbo + rt * 8192, ws + WS_SLAB); gld_sc1<1024>(pp[rt][1], pbo + rt * 8192, ws + WS_SLAB);
    });
  };
  wait_bar<63>();
  const char *sxb = nullptr, *swb = G.wbase(1);
  u32x4 ov;
  G.run(acc, [&](auto S, auto I) {
    constexpr int s = decltype(S)::value, i = decltype(I)::value;
    if constexpr (i == 0 && ((s & 1) == 0 || s == 5) && s < 6) fl_load();
    if constexpr (s == 0) {
      if constexpr (i < 4) G.template issue_w<1, i>(swb);
      if constexpr (i >= 5 && i < 13) {
        constexpr int k = i - 5;
        __builtin_amdgcn_raw_buffer_store_b128(
            ov, rsO, ob + (32 * w + 8 * (k & 3) + (ln >> 3)) * 256 + (k >> 2) * 128 + (((ln & 7) ^ ((4 * (k & 3) + g2) & 7)) << 4), 0, 16);
      }
      if constexpr (i >= 4 && i < 12) ov = *(const LDS_AS u32x4*)(lds0 + XB[(i - 4) >> 2] + (4 * w + ((i - 4) & 3)) * 1024 + ln * 16);
    }
    if constexpr (i == 15 && s < 7) {
      wait_bar<0>();
      if constexpr (s == 0) {
        if (threadIdx.x == 0)
          __hip_atomic_store((gu64*)(flagO + ((p * NB + b) * NH + h) * 8), tag, __ATOMIC_RELAXED, __HIP_MEMORY_SCOPE_AGENT);
      }
      if constexpr ((s & 1) == 0 && s < 6) { if (!fl_bit((s + 2) >> 1)) fl_spin((s + 2) >> 1); }
      if constexpr (s == 5) { const bool pr = fl_bit(4); early = pr && half; }
      if constexpr (s == 6) { if (early) pp_load(); }
      sxb = G.xbase(s + 2); swb = G.wbase(s + 2);
    }
    constexpr int t = i >= 15 ? s + 2 : s + 1, n = i >= 15 ? i - 15 : i + 3;
    if constexpr (n < 9 && (i >= 15 ? s < 7 : s >= 1) && t < 8) G.template issue<t, n>(swb, sxb);
  });
  if (half == 0) {
#pragma unroll
    for (int rt = 0; rt < 9; ++rt) {
      __builtin_amdgcn_raw_buffer_store_b128(__builtin_bit_cast(u32x4, acc[rt][0]), rsS, pbo + rt * 8192, 0, 16);
      __builtin_amdgcn_raw_buffer_store_b128(__builtin_bit_cast(u32x4, acc[rt][1]), rsS, pbo + rt * 8192 + 1024, 0, 16);
    }
    publish(flagP + (bid & 255) * 8, tag);
  } else {
    if (!early) { fl_spin(4); pp_load(); }
    asm volatile("s_waitcnt vmcnt(0)"
                 : "+v"(pp[0][0]), "+v"(pp[0][1]), "+v"(pp[1][0]), "+v"(pp[1][1]), "+v"(pp[2][0]), "+v"(pp[2][1]), "+v"(pp[3][0]), "+v"(pp[3][1]),
                   "+v"(pp[4][0]), "+v"(pp[4][1]), "+v"(pp[5][0]), "+v"(pp[5][1]), "+v"(pp[6][0]), "+v"(pp[6][1]), "+v"(pp[7][0]), "+v"(pp[7][1]),
                   "+v"(pp[8][0]), "+v"(pp[8][1]));
    const int tile = (p * NB + b) * 4 + nt;
    const int own0 = hq == 0 ? 0 : 2 * hq + 1, own1 = 2 * hq + 3;
    const unsigned sb = (256u + tile * 4) * 73728 + w * 2048 + ln * 16;
#pragma unroll
    for (int rt = 0; rt < 9; ++rt) {
      acc[rt][0] = __builtin_bit_cast(f32x4, pp[rt][0]) + acc[rt][0];
      acc[rt][1] = __builtin_bit_cast(f32x4, pp[rt][1]) + acc[rt][1];
      if (rt < own0 || rt >= own1) {
        __builtin_amdgcn_raw_buffer_store_b128(__builtin_bit_cast(u32x4, acc[rt][0]), rsS, sb + hq * 73728 + rt * 8192, 0, 16);
        __builtin_amdgcn_raw_buffer_store_b128(__builtin_bit_cast(u32x4, acc[rt][1]), rsS, sb + hq * 73728 + rt * 8192 + 1024, 0, 16);
      }
    }
    publish(flagS + (tile * 4 + hq) * 8, tag);
    await(flagS + tile * 32, 8, 4, tag, w, ln);
    u16* orow = out + (size_t)(b * SQ + c2) * (2 * DM) + p * DM + nt * 128 + 32 * w + 8 * g2;
#pragma unroll
    for (int rt = 0; rt < 9; ++rt)
      if (rt >= own0 && rt < own1) {
        f32x4 t0[4], t1[4];
#pragma unroll
        for (int k = 0; k < 4; ++k) {
          const unsigned a = sb + (k == hq ? 0x7f000000u : k * 73728 + rt * 8192);
          t0[k] = __builtin_bit_cast(f32x4, __builtin_amdgcn_raw_buffer_load_b128(rsS, a, 0, 16));
          t1[k] = __builtin_bit_cast(f32x4, __builtin_amdgcn_raw_buffer_load_b128(rsS, a + 1024, 0, 16));
        }
        f32x4 s0 = f32x4{}, s1 = f32x4{};
#pragma unroll
        for (int k = 0; k < 4; ++k) {
          s0 += k == hq ? acc[rt][0] : t0[k];
          s1 += k == hq ? acc[rt][1] : t1[k];
        }
        *(u32x4*)(orow + (size_t)(16 * rt) * (2 * DM)) = c2frag(pk4(s0), pk4(s1));
      }
  }
}

extern "C" void kernel_launch(void* const* d_in, const int* in_sizes, int n_in, void* d_out, int out_size, void* d_ws, size_t ws_size,
                              hipStream_t stream) {
  (void)in_sizes; (void)n_in; (void)out_size; (void)ws_size;
  const u16* const* in = (const u16* const*)d_in;
  (void)hipFuncSetAttribute((const void*)mha_fused, hipFuncAttributeMaxDynamicSharedMemorySize, LDS_BYTES);
  mha_fused<<<512, 256, LDS_BYTES, stream>>>(in[0], in[1], in[2], in[3], in[4], in[5], in[6], in[7], in[8], in[9], (char*)d_ws,
                                             (u16*)d_out);
}
```
